```python
import torch

def _softmax(x, axis=-1):
    return torch.softmax(x, dim=axis)
def _gelu(x):
    return torch.nn.functional.gelu(x, approximate="tanh")
def _silu(x):
    return torch.nn.functional.silu(x)
def _sigmoid(x):
    return torch.sigmoid(x)
def _layernorm(x, axis=-1, eps=1e-5):
    m = x.mean(dim=axis, keepdim=True)
    v = x.var(dim=axis, keepdim=True, unbiased=False)
    return (x - m) / torch.sqrt(v + eps)
def _scatter_add(tgt, idx, src, axis=0):
    return tgt.clone().index_add_(axis, idx, src)
def _segment_sum(src, seg, n):
    out = torch.zeros((n,) + src.shape[1:], dtype=src.dtype)
    return out.index_add_(0, seg, src)
def _pad(x, pw):
    flat = tuple(v for pair in reversed(pw) for v in pair)
    return torch.nn.functional.pad(x, flat)

kernel_name = 'standalone__gqa_softmax__near_ridge'

def setup_inputs():

    _w = (lambda a: (lambda t: t.to(torch.bfloat16) if t.is_floating_point() else t)(torch.as_tensor(a)))
    B, Hq, Hkv, Sq, Sk, D = 8, 32, 32, 288, 512, 128
    return {
        "q": _w((lambda *s: torch.randn(*s, dtype=torch.bfloat16) * 0.8)(B, Hq, Sq, D)),
        "k": _w((lambda *s: torch.randn(*s, dtype=torch.bfloat16) * 0.8)(B, Hkv, Sk, D)),
        "v": _w((lambda *s: torch.randn(*s, dtype=torch.bfloat16) * 0.05)(B, Hkv, Sk, D)),
    }

def reference(q, k, v):
    B, Hq, Sq, D = q.shape
    Hkv = k.shape[1]
    rep = Hq // Hkv
    k = (lambda x, n, axis: torch.repeat_interleave(x, n, dim=axis))(k, rep, axis=1)
    v = (lambda x, n, axis: torch.repeat_interleave(x, n, dim=axis))(v, rep, axis=1)
    scale = D ** -0.5
    scores = torch.einsum("bhqd,bhkd->bhqk", q, k) * scale
    p = _softmax(scores, axis=-1)
    return torch.einsum("bhqk,bhkd->bhqd", p, v)
```

```cpp
#include <hip/hip_runtime.h>
#include <hip/hip_bf16.h>
typedef unsigned u32x4 __attribute__((ext_vector_type(4)));
typedef unsigned u32x2 __attribute__((ext_vector_type(2)));
typedef __bf16 bf16x8 __attribute__((ext_vector_type(8)));
typedef __bf16 bf16x2 __attribute__((ext_vector_type(2)));
typedef short s16x4 __attribute__((ext_vector_type(4)));
typedef float f32x16 __attribute__((ext_vector_type(16)));
typedef float f32x4 __attribute__((ext_vector_type(4)));
typedef float f32x2 __attribute__((ext_vector_type(2)));
#define DI __device__ __forceinline__
constexpr int SQ = 288, SK = 512, D = 128;
constexpr float CSC = 0.088388347648318440f * 1.4426950408889634f;

DI unsigned cvtpk(float lo, float hi) { f32x2 v = {lo, hi}; bf16x2 b = __builtin_convertvector(v, bf16x2); return __builtin_bit_cast(unsigned, b); }
DI float swap32max(float x) { auto r = __builtin_amdgcn_permlane32_swap(__float_as_uint(x), __float_as_uint(x), false, false); return fmaxf(__uint_as_float(r[0]), __uint_as_float(r[1])); }
DI float swap32add(float x) { auto r = __builtin_amdgcn_permlane32_swap(__float_as_uint(x), __float_as_uint(x), false, false); return __uint_as_float(r[0]) + __uint_as_float(r[1]); }

DI void dma_slot(const char* g, unsigned ldsa, int w, unsigned ve, unsigned vo) {
  if (w < 8) {
#pragma unroll
  for (int i = 0; i < 4; ++i) {
    const int p = 4 * w + i, R = p >> 1, m = i & 1;
    const char* gb = g + 2048 * R + 128 * m;
    const unsigned m0v = ldsa + 1024u * p;
    asm volatile("s_mov_b32 m0, %0\n\ts_nop 0\n\tglobal_load_lds_dwordx4 %1, %2" :: "s"(m0v), "v"(((i >> 1) & 1) ? vo : ve), "s"(gb) : "memory");
  }
  }
}
DI bf16x8 prescale(u32x4 r) {
  u32x4 o;
#pragma unroll
  for (int e = 0; e < 4; ++e) o[e] = cvtpk(__uint_as_float(r[e] << 16) * CSC, __uint_as_float(r[e] & 0xffff0000u) * CSC);
  return __builtin_bit_cast(bf16x8, o);
}
struct Blk { f32x16 o[4]; float m, l; };
DI void qk_sm(Blk& b, const bf16x8 (&qf)[8], const bf16x8 (&kf)[8], bool first, bf16x8& P0, bf16x8& P1) {
  f32x16 s = {};
#pragma unroll
  for (int d0 = 0; d0 < 8; ++d0) s = __builtin_amdgcn_mfma_f32_32x32x16_bf16(kf[d0], qf[d0], s, 0, 0, 0);
  float mx = s[0];
#pragma unroll
  for (int r = 1; r < 16; ++r) mx = fmaxf(mx, s[r]);
  mx = swap32max(mx);
  if (first) { b.m = mx; }
  else if (__any(mx > b.m + 8.f)) {
    const float mn = fmaxf(b.m, mx), al = __builtin_amdgcn_exp2f(b.m - mn);
    b.m = mn; b.l *= al;
#pragma unroll
    for (int db = 0; db < 4; ++db)
#pragma unroll
      for (int r = 0; r < 16; ++r) b.o[db][r] *= al;
  }
  float rs = 0.f;
#pragma unroll
  for (int r = 0; r < 16; ++r) { s[r] = __builtin_amdgcn_exp2f(s[r] - b.m); rs += s[r]; }
  b.l += rs;
  u32x4 p0 = {cvtpk(s[0], s[1]), cvtpk(s[2], s[3]), cvtpk(s[4], s[5]), cvtpk(s[6], s[7])};
  u32x4 p1 = {cvtpk(s[8], s[9]), cvtpk(s[10], s[11]), cvtpk(s[12], s[13]), cvtpk(s[14], s[15])};
  P0 = __builtin_bit_cast(bf16x8, p0); P1 = __builtin_bit_cast(bf16x8, p1);
}
DI void pv(Blk& b, const bf16x8& P0, const bf16x8& P1, const bf16x8 (&vf)[8]) {
#pragma unroll
  for (int db = 0; db < 4; ++db) {
    b.o[db] = __builtin_amdgcn_mfma_f32_32x32x16_bf16(vf[2 * db], P0, b.o[db], 0, 0, 0);
    b.o[db] = __builtin_amdgcn_mfma_f32_32x32x16_bf16(vf[2 * db + 1], P1, b.o[db], 0, 0, 0);
  }
}
template <int OFF> DI s16x4 tr_read(unsigned vb) { s16x4 r; asm volatile("ds_read_b64_tr_b16 %0, %1 offset:%2" : "=&v"(r) : "v"(vb), "i"(OFF) : "memory"); return r; }

__global__ __launch_bounds__(576) void attn_v0(const char* __restrict__ q, const char* __restrict__ k, const char* __restrict__ v, char* __restrict__ o) {
  extern __shared__ __attribute__((aligned(16))) char lds[];
  const int h = blockIdx.x, t = threadIdx.x, lane = t & 63, w = __builtin_amdgcn_readfirstlane(t >> 6);
  const char* qp = q + (size_t)h * (SQ * 256);
  const char* kp = k + (size_t)h * (SK * 256);
  const char* vp = v + (size_t)h * (SK * 256);
  char* op = o + (size_t)h * (SQ * 256);
  const int r32 = lane & 31, hi = lane >> 5;
  u32x4 qr[8];
  const unsigned ldsb = (unsigned)(size_t)lds;
  const unsigned dve = 256u * ((lane >> 2) & 7) + 64u * (lane >> 5) + 16u * ((lane & 3) ^ (((lane >> 2) & 7) >> 2));
  const unsigned dvo = 256u * ((lane >> 2) & 7) + 64u * (lane >> 5) + 16u * ((lane & 3) ^ (2 + (((lane >> 2) & 7) >> 2)));
#pragma unroll
  for (int i = 0; i < 8; ++i) {
    const int off = (32 * w + r32) * 256 + (2 * i + hi) * 16;
    asm volatile("global_load_dwordx4 %0, %1, %2" : "=v"(qr[i]) : "v"(off), "s"(qp) : "memory");
  }
  dma_slot(kp, ldsb, w, dve, dvo);
  dma_slot(vp, ldsb + 32768, w, dve, dvo);
  dma_slot(kp + 32768, ldsb + 65536, w, dve, dvo);
  dma_slot(vp + 32768, ldsb + 98304, w, dve, dvo);
  dma_slot(kp + 65536, ldsb + 131072, w, dve, dvo);
  if (w < 8) asm volatile("s_waitcnt vmcnt(20)" ::: "memory"); else asm volatile("s_waitcnt vmcnt(0)" ::: "memory");
#pragma unroll
  for (int i = 0; i < 8; ++i) asm volatile("" : "+v"(qr[i]));
  __builtin_amdgcn_sched_barrier(0);
  bf16x8 qa[8];
#pragma unroll
  for (int i = 0; i < 8; ++i) qa[i] = prescale(qr[i]);
  Blk A;
#pragma unroll
  for (int db = 0; db < 4; ++db) A.o[db] = f32x16{};
  A.l = 0.f; A.m = 0.f;
  const unsigned kr0 = 2048u * (r32 >> 3) + 64u * (r32 & 7) + 16u * ((0u + hi) ^ ((r32 >> 2) & 3));
  const unsigned kr1 = 2048u * (r32 >> 3) + 64u * (r32 & 7) + 16u * ((2u + hi) ^ ((r32 >> 2) & 3));
  const unsigned g = lane >> 4, dsub = g & 1, qq = (lane & 15) >> 2, pp = lane & 3;
  const unsigned vr0 = 64u * (4 * hi + qq) + 16u * ((2 * dsub + (pp >> 1)) ^ (0u + hi)) + 8u * (pp & 1);
  const unsigned vr1 = 2048u + 64u * (4 * hi + qq) + 16u * ((2 * dsub + (pp >> 1)) ^ (2u + hi)) + 8u * (pp & 1);
#pragma unroll 1
  for (int T = 0; T < 4; ++T) {
    if (T == 2) asm volatile("s_waitcnt vmcnt(8)" ::: "memory");
    else if (T == 3) asm volatile("s_waitcnt vmcnt(0)" ::: "memory");
    else asm volatile("s_waitcnt vmcnt(12)" ::: "memory");
    __builtin_amdgcn_s_barrier();
    const unsigned ks = (T == 0 ? 0 : T == 1 ? 2 : T == 2 ? 4 : 1) * 32768u, vs = (T == 0 ? 1 : T == 1 ? 3 : T == 2 ? 0 : 2) * 32768u;
#pragma unroll 1
    for (int i = 0; i < 4; ++i) {
      const unsigned kb = (unsigned)i * 8192u;
      bf16x8 kf[8], vf[8];
#pragma unroll
      for (int d0 = 0; d0 < 8; ++d0) kf[d0] = *(const bf16x8*)(lds + ks + kb + ((d0 & 1) ? kr1 : kr0) + 512 * (d0 >> 1));
      const unsigned vb0 = ldsb + vs + kb + vr0, vb1 = ldsb + vs + kb + vr1;
      s16x4 lo[8], up[8];
      lo[0] = tr_read<0>(vb0); up[0] = tr_read<0>(vb1); lo[1] = tr_read<4096>(vb0); up[1] = tr_read<4096>(vb1);
      lo[2] = tr_read<512>(vb0); up[2] = tr_read<512>(vb1); lo[3] = tr_read<4096 + 512>(vb0); up[3] = tr_read<4096 + 512>(vb1);
      lo[4] = tr_read<1024>(vb0); up[4] = tr_read<1024>(vb1); lo[5] = tr_read<4096 + 1024>(vb0); up[5] = tr_read<4096 + 1024>(vb1);
      lo[6] = tr_read<1536>(vb0); up[6] = tr_read<1536>(vb1); lo[7] = tr_read<4096 + 1536>(vb0); up[7] = tr_read<4096 + 1536>(vb1);
      const bool first = (T == 0 && i == 0);
      bf16x8 PA0, PA1;
      qk_sm(A, qa, kf, first, PA0, PA1);
      asm volatile("s_waitcnt lgkmcnt(0)" : "+v"(lo[0]), "+v"(lo[1]), "+v"(lo[2]), "+v"(lo[3]), "+v"(lo[4]), "+v"(lo[5]), "+v"(lo[6]), "+v"(lo[7]),
                   "+v"(up[0]), "+v"(up[1]), "+v"(up[2]), "+v"(up[3]), "+v"(up[4]), "+v"(up[5]), "+v"(up[6]), "+v"(up[7]) :: "memory");
#pragma unroll
      for (int j = 0; j < 8; ++j) { auto x = __builtin_shufflevector(lo[j], up[j], 0, 1, 2, 3, 4, 5, 6, 7); vf[j] = __builtin_bit_cast(bf16x8, x); }
      pv(A, PA0, PA1, vf);
    }
    asm volatile("s_waitcnt lgkmcnt(0)" ::: "memory");
    __builtin_amdgcn_s_barrier();
    if (T == 0) { dma_slot(vp + 65536, ldsb, w, dve, dvo); dma_slot(kp + 98304, ldsb + 32768, w, dve, dvo); }
    else if (T == 1) dma_slot(vp + 98304, ldsb + 65536, w, dve, dvo);
  }
  {
    Blk& b = A;
    const float inv = __builtin_amdgcn_rcpf(swap32add(b.l));
    char* orow = op + (32 * w + r32) * 256;
#pragma unroll
    for (int db = 0; db < 4; ++db)
#pragma unroll
      for (int gq = 0; gq < 4; gq += 2) {
        unsigned a0 = cvtpk(b.o[db][4 * gq] * inv, b.o[db][4 * gq + 1] * inv), a1 = cvtpk(b.o[db][4 * gq + 2] * inv, b.o[db][4 * gq + 3] * inv);
        unsigned b0 = cvtpk(b.o[db][4 * gq + 4] * inv, b.o[db][4 * gq + 5] * inv), b1 = cvtpk(b.o[db][4 * gq + 6] * inv, b.o[db][4 * gq + 7] * inv);
        auto r0 = __builtin_amdgcn_permlane32_swap(a0, b0, false, false); auto r1 = __builtin_amdgcn_permlane32_swap(a1, b1, false, false);
        u32x4 x = {r0[0], r1[0], r0[1], r1[1]};
        __builtin_nontemporal_store(x, (u32x4*)(orow + (32 * db + 8 * gq) * 2 + 16 * hi));
      }
  }
}
extern "C" void kernel_launch(void* const* d_in, const int* in_sizes, int n_in, void* d_out, int out_size, void* d_ws, size_t ws_size, hipStream_t stream) {
  (void)hipFuncSetAttribute((const void*)attn_v0, hipFuncAttributeMaxDynamicSharedMemorySize, 163840);
  attn_v0<<<256, 576, 163840, stream>>>((const char*)d_in[0], (const char*)d_in[1], (const char*)d_in[2], (char*)d_out);
}
```

```cpp
#include <hip/hip_runtime.h>
#include <hip/hip_bf16.h>
#define STAMPS 0
typedef unsigned u32x4 __attribute__((ext_vector_type(4)));
typedef unsigned u32x2 __attribute__((ext_vector_type(2)));
typedef __bf16 bf16x8 __attribute__((ext_vector_type(8)));
typedef __bf16 bf16x2 __attribute__((ext_vector_type(2)));
typedef short s16x4 __attribute__((ext_vector_type(4)));
typedef float f32x16 __attribute__((ext_vector_type(16)));
typedef float f32x2 __attribute__((ext_vector_type(2)));
#define DI __device__ __forceinline__
namespace gqa {
constexpr int kSq = 288, kSk = 512, kHq = 32, kHkv = 32, kB = 8;
constexpr float kC = 0.088388347648318440f * 1.4426950408889634f;

DI unsigned cvtpk(float lo, float hi) { f32x2 v = {lo, hi}; bf16x2 b = __builtin_convertvector(v, bf16x2); return __builtin_bit_cast(unsigned, b); }
DI float swapmax(float x) { auto r = __builtin_amdgcn_permlane32_swap(__float_as_uint(x), __float_as_uint(x), false, false); return fmaxf(__uint_as_float(r[0]), __uint_as_float(r[1])); }
DI float swapadd(float x) { auto r = __builtin_amdgcn_permlane32_swap(__float_as_uint(x), __float_as_uint(x), false, false); return __uint_as_float(r[0]) + __uint_as_float(r[1]); }
template <int OFF> DI s16x4 tr_read(unsigned vb) { s16x4 r; asm volatile("ds_read_b64_tr_b16 %0, %1 offset:%2\n\ts_waitcnt lgkmcnt(0)" : "=&v"(r) : "v"(vb), "i"(OFF) : "memory"); return r; }

DI void slow_block(const char* qp, const char* kp, const char* vp, char* op, int row0, char* lds, unsigned ldsa, int lane) {
  const int r32 = lane & 31, hi = lane >> 5;
  bf16x8 qf[8];
#pragma unroll
  for (int d0 = 0; d0 < 8; ++d0) qf[d0] = *(const bf16x8*)(qp + (row0 + r32) * 256 + (2 * d0 + hi) * 16);
  f32x16 o[4];
#pragma unroll
  for (int db = 0; db < 4; ++db) o[db] = f32x16{};
  float m = -__builtin_inff(), l = 0.f;
  const unsigned g = lane >> 4, dsub = g & 1, qq = (lane & 15) >> 2, pp = lane & 3;
  const unsigned vr0 = ldsa + 64u * (4 * hi + qq) + 16u * ((2 * dsub + (pp >> 1)) ^ (0u + hi)) + 8u * (pp & 1);
  const unsigned vr1 = ldsa + 2048u + 64u * (4 * hi + qq) + 16u * ((2 * dsub + (pp >> 1)) ^ (2u + hi)) + 8u * (pp & 1);
#pragma unroll 1
  for (int u = 0; u < 16; ++u) {
    f32x16 s = {};
#pragma unroll
    for (int d0 = 0; d0 < 8; ++d0) {
      const bf16x8 kf = *(const bf16x8*)(kp + (32 * u + r32) * 256 + (2 * d0 + hi) * 16);
      s = __builtin_amdgcn_mfma_f32_32x32x16_bf16(kf, qf[d0], s, 0, 0, 0);
    }
#pragma unroll
    for (int i = 0; i < 8; ++i) {
      const int n = lane + 64 * i, row = n >> 4, ch = n & 15;
      const u32x4 x = *(const u32x4*)(vp + (32 * u + row) * 256 + ch * 16);
      *(u32x4*)(lds + 2048 * (row >> 3) + 512 * (ch >> 2) + 64 * (row & 7) + 16 * ((ch & 3) ^ ((row >> 2) & 3))) = x;
    }
    float mx = -__builtin_inff();
#pragma unroll
    for (int r = 0; r < 16; ++r) { s[r] *= kC; mx = fmaxf(mx, s[r]); }
    mx = swapmax(mx);
    const float mn = fmaxf(m, mx), al = __builtin_amdgcn_exp2f(m - mn);
    m = mn; l *= al;
#pragma unroll
    for (int db = 0; db < 4; ++db)
#pragma unroll
      for (int r = 0; r < 16; ++r) o[db][r] *= al;
    float nanp = 0.f;
#pragma unroll
    for (int r = 0; r < 16; ++r) { const float x = s[r]; s[r] = __builtin_amdgcn_exp2f(x - m); l += s[r]; }
    (void)nanp;
    const u32x4 p0 = {cvtpk(s[0], s[1]), cvtpk(s[2], s[3]), cvtpk(s[4], s[5]), cvtpk(s[6], s[7])};
    const u32x4 p1 = {cvtpk(s[8], s[9]), cvtpk(s[10], s[11]), cvtpk(s[12], s[13]), cvtpk(s[14], s[15])};
    const bf16x8 P0 = __builtin_bit_cast(bf16x8, p0), P1 = __builtin_bit_cast(bf16x8, p1);
    asm volatile("s_waitcnt lgkmcnt(0)" ::: "memory");
#define GQA_PV(DB) { \
      s16x4 a0 = tr_read<512 * DB>(vr0), a1 = tr_read<512 * DB>(vr1), b0 = tr_read<4096 + 512 * DB>(vr0), b1 = tr_read<4096 + 512 * DB>(vr1); \
      __builtin_amdgcn_sched_barrier(0); \
      o[DB] = __builtin_amdgcn_mfma_f32_32x32x16_bf16(__builtin_bit_cast(bf16x8, __builtin_shufflevector(a0, a1, 0, 1, 2, 3, 4, 5, 6, 7)), P0, o[DB], 0, 0, 0); \
      o[DB] = __builtin_amdgcn_mfma_f32_32x32x16_bf16(__builtin_bit_cast(bf16x8, __builtin_shufflevector(b0, b1, 0, 1, 2, 3, 4, 5, 6, 7)), P1, o[DB], 0, 0, 0); }
    GQA_PV(0) GQA_PV(1) GQA_PV(2) GQA_PV(3)
#undef GQA_PV
  }
  const float inv = 1.0f / swapadd(l);
  char* orow = op + (row0 + r32) * 256;
#pragma unroll
  for (int db = 0; db < 4; ++db)
#pragma unroll
    for (int gq = 0; gq < 4; ++gq) {
      const u32x2 x = {cvtpk(o[db][4 * gq] * inv, o[db][4 * gq + 1] * inv), cvtpk(o[db][4 * gq + 2] * inv, o[db][4 * gq + 3] * inv)};
      *(u32x2*)(orow + (32 * db + 8 * gq + 4 * hi) * 2) = x;
    }
}

__global__ __launch_bounds__(256, 1) void attn_kernel(const char* __restrict__ q, const char* __restrict__ k, const char* __restrict__ v, char* __restrict__ o) {
  extern __shared__ __attribute__((aligned(256))) char lds[];
  unsigned long long t0; asm volatile("s_memrealtime %0\n\ts_waitcnt lgkmcnt(0)" : "=s"(t0));
  const unsigned bh = blockIdx.x;
  const unsigned w = __builtin_amdgcn_readfirstlane(threadIdx.x >> 6);
  const char* qa = q + (size_t)bh * (kSq * 256); const char* ka = k + (size_t)bh * (kSk * 256); const char* va = v + (size_t)bh * (kSk * 256); char* oa = o + (size_t)bh * (kSq * 256);
  const unsigned ldsb = (unsigned)(size_t)lds;
  asm volatile("v_mov_b32 v255, 0\n\tv_accvgpr_write_b32 a255, v255" ::: "v255", "a255");
  int lane = threadIdx.x & 63;
  char* my = lds + w * 8192;
  const unsigned mya = ldsb + w * 8192;
  const int nb = w == 0 ? 3 : 2;
#pragma unroll 1
  for (int b = 0; b < nb; ++b) slow_block(qa, ka, va, oa, b < 2 ? 64 * (int)w + 32 * b : 256, my, mya, lane);
  asm volatile("s_waitcnt vmcnt(0)\n\tbuffer_wbl2 sc0 sc1\n\ts_waitcnt vmcnt(0)" ::: "memory");
  for (;;) { unsigned long long t; asm volatile("s_memrealtime %0\n\ts_waitcnt lgkmcnt(0)" : "=s"(t)); if ((unsigned)t - (unsigned)t0 >= 30000u) break; }
}
}
extern "C" void kernel_launch(void* const* d_in, const int* in_sizes, int n_in, void* d_out, int out_size, void* d_ws, size_t ws_size, hipStream_t stream) {
  (void)hipFuncSetAttribute((const void*)gqa::attn_kernel, hipFuncAttributeMaxDynamicSharedMemorySize, 163840);
  hipLaunchKernelGGL(gqa::attn_kernel, dim3(256), dim3(256), 163840, stream, (const char*)d_in[0], (const char*)d_in[1], (const char*)d_in[2], (char*)d_out);
}
```

```cpp
#include <hip/hip_runtime.h>
#include <hip/hip_bf16.h>
#define STAMPS 0
typedef unsigned u32x4 __attribute__((ext_vector_type(4)));
typedef unsigned u32x2 __attribute__((ext_vector_type(2)));
typedef __bf16 bf16x8 __attribute__((ext_vector_type(8)));
typedef __bf16 bf16x2 __attribute__((ext_vector_type(2)));
typedef short s16x4 __attribute__((ext_vector_type(4)));
typedef float f32x16 __attribute__((ext_vector_type(16)));
typedef float f32x2 __attribute__((ext_vector_type(2)));
#define DI __device__ __forceinline__
namespace gqa {
constexpr int kSq = 288, kSk = 512, kHq = 32, kHkv = 32, kB = 8;
constexpr float kC = 0.088388347648318440f * 1.4426950408889634f;

DI unsigned cvtpk(float lo, float hi) { f32x2 v = {lo, hi}; bf16x2 b = __builtin_convertvector(v, bf16x2); return __builtin_bit_cast(unsigned, b); }
DI float swapmax(float x) { auto r = __builtin_amdgcn_permlane32_swap(__float_as_uint(x), __float_as_uint(x), false, false); return fmaxf(__uint_as_float(r[0]), __uint_as_float(r[1])); }
DI float swapadd(float x) { auto r = __builtin_amdgcn_permlane32_swap(__float_as_uint(x), __float_as_uint(x), false, false); return __uint_as_float(r[0]) + __uint_as_float(r[1]); }
template <int OFF> DI s16x4 tr_read(unsigned vb) { s16x4 r; asm volatile("ds_read_b64_tr_b16 %0, %1 offset:%2\n\ts_waitcnt lgkmcnt(0)" : "=&v"(r) : "v"(vb), "i"(OFF) : "memory"); return r; }

DI void slow_block(const char* qp, const char* kp, const char* vp, char* op, int row0, char* lds, unsigned ldsa, int lane) {
  const int r32 = lane & 31, hi = lane >> 5;
  bf16x8 qf[8];
#pragma unroll
  for (int d0 = 0; d0 < 8; ++d0) qf[d0] = *(const bf16x8*)(qp + (row0 + r32) * 256 + (2 * d0 + hi) * 16);
  f32x16 o[4];
#pragma unroll
  for (int db = 0; db < 4; ++db) o[db] = f32x16{};
  float m = -__builtin_inff(), l = 0.f;
  const unsigned g = lane >> 4, dsub = g & 1, qq = (lane & 15) >> 2, pp = lane & 3;
  const unsigned vr0 = ldsa + 64u * (4 * hi + qq) + 16u * ((2 * dsub + (pp >> 1)) ^ (0u + hi)) + 8u * (pp & 1);
  const unsigned vr1 = ldsa + 2048u + 64u * (4 * hi + qq) + 16u * ((2 * dsub + (pp >> 1)) ^ (2u + hi)) + 8u * (pp & 1);
#pragma unroll 1
  for (int u = 0; u < 16; ++u) {
    f32x16 s = {};
#pragma unroll
    for (int d0 = 0; d0 < 8; ++d0) {
      const bf16x8 kf = *(const bf16x8*)(kp + (32 * u + r32) * 256 + (2 * d0 + hi) * 16);
      s = __builtin_amdgcn_mfma_f32_32x32x16_bf16(kf, qf[d0], s, 0, 0, 0);
    }
#pragma unroll
    for (int i = 0; i < 8; ++i) {
      const int n = lane + 64 * i, row = n >> 4, ch = n & 15;
      const u32x4 x = *(const u32x4*)(vp + (32 * u + row) * 256 + ch * 16);
      *(u32x4*)(lds + 2048 * (row >> 3) + 512 * (ch >> 2) + 64 * (row & 7) + 16 * ((ch & 3) ^ ((row >> 2) & 3))) = x;
    }
    float mx = -__builtin_inff();
#pragma unroll
    for (int r = 0; r < 16; ++r) { s[r] *= kC; mx = fmaxf(mx, s[r]); }
    mx = swapmax(mx);
    const float mn = fmaxf(m, mx), al = __builtin_amdgcn_exp2f(m - mn);
    m = mn; l *= al;
#pragma unroll
    for (int db = 0; db < 4; ++db)
#pragma unroll
      for (int r = 0; r < 16; ++r) o[db][r] *= al;
    float nanp = 0.f;
#pragma unroll
    for (int r = 0; r < 16; ++r) { const float x = s[r]; s[r] = __builtin_amdgcn_exp2f(x - m); l += s[r]; }
    (void)nanp;
    const u32x4 p0 = {cvtpk(s[0], s[1]), cvtpk(s[2], s[3]), cvtpk(s[4], s[5]), cvtpk(s[6], s[7])};
    const u32x4 p1 = {cvtpk(s[8], s[9]), cvtpk(s[10], s[11]), cvtpk(s[12], s[13]), cvtpk(s[14], s[15])};
    const bf16x8 P0 = __builtin_bit_cast(bf16x8, p0), P1 = __builtin_bit_cast(bf16x8, p1);
    asm volatile("s_waitcnt lgkmcnt(0)" ::: "memory");
#define GQA_PV(DB) { \
      s16x4 a0 = tr_read<512 * DB>(vr0), a1 = tr_read<512 * DB>(vr1), b0 = tr_read<4096 + 512 * DB>(vr0), b1 = tr_read<4096 + 512 * DB>(vr1); \
      __builtin_amdgcn_sched_barrier(0); \
      o[DB] = __builtin_amdgcn_mfma_f32_32x32x16_bf16(__builtin_bit_cast(bf16x8, __builtin_shufflevector(a0, a1, 0, 1, 2, 3, 4, 5, 6, 7)), P0, o[DB], 0, 0, 0); \
      o[DB] = __builtin_amdgcn_mfma_f32_32x32x16_bf16(__builtin_bit_cast(bf16x8, __builtin_shufflevector(b0, b1, 0, 1, 2, 3, 4, 5, 6, 7)), P1, o[DB], 0, 0, 0); }
    GQA_PV(0) GQA_PV(1) GQA_PV(2) GQA_PV(3)
#undef GQA_PV
  }
  const float inv = 1.0f / swapadd(l);
  char* orow = op + (row0 + r32) * 256;
#pragma unroll
  for (int db = 0; db < 4; ++db)
#pragma unroll
    for (int gq = 0; gq < 4; ++gq) {
      const u32x2 x = {cvtpk(o[db][4 * gq] * inv, o[db][4 * gq + 1] * inv), cvtpk(o[db][4 * gq + 2] * inv, o[db][4 * gq + 3] * inv)};
      *(u32x2*)(orow + (32 * db + 8 * gq + 4 * hi) * 2) = x;
    }
}

__global__ __launch_bounds__(256, 1) void attn_kernel(const char* __restrict__ q, const char* __restrict__ k, const char* __restrict__ v, char* __restrict__ o) {
  extern __shared__ __attribute__((aligned(256))) char lds[];
  unsigned long long t0; asm volatile("s_memrealtime %0\n\ts_waitcnt lgkmcnt(0)" : "=s"(t0));
  for (unsigned bh = blockIdx.x; bh < 256; bh += gridDim.x) {
  const unsigned w = __builtin_amdgcn_readfirstlane(threadIdx.x >> 6);
  const char* qa = q + (size_t)bh * (kSq * 256); const char* ka = k + (size_t)bh * (kSk * 256); const char* va = v + (size_t)bh * (kSk * 256); char* oa = o + (size_t)bh * (kSq * 256);
  const unsigned ldsb = (unsigned)(size_t)lds;
  int lane = threadIdx.x & 63;
  char* my = lds + w * 8192;
  const unsigned mya = ldsb + w * 8192;
  const int nb = w == 0 ? 3 : 2;
#pragma unroll 1
  for (int b = 0; b < nb; ++b) slow_block(qa, ka, va, oa, b < 2 ? 64 * (int)w + 32 * b : 256, my, mya, lane);
  }
  for (;;) { unsigned long long t; asm volatile("s_memrealtime %0\n\ts_waitcnt lgkmcnt(0)" : "=s"(t)); if ((unsigned)t - (unsigned)t0 >= 60000u) break; }
}
}
extern "C" void kernel_launch(void* const* d_in, const int* in_sizes, int n_in, void* d_out, int out_size, void* d_ws, size_t ws_size, hipStream_t stream) {
  (void)hipFuncSetAttribute((const void*)gqa::attn_kernel, hipFuncAttributeMaxDynamicSharedMemorySize, 163840);
  hipLaunchKernelGGL(gqa::attn_kernel, dim3(256), dim3(256), 32768, stream, (const char*)d_in[0], (const char*)d_in[1], (const char*)d_in[2], (char*)d_out);
}
```

```cpp
#include <hip/hip_runtime.h>
#include <hip/hip_bf16.h>
typedef unsigned u32x4 __attribute__((ext_vector_type(4)));
typedef unsigned u32x2 __attribute__((ext_vector_type(2)));
typedef __bf16 bf16x8 __attribute__((ext_vector_type(8)));
typedef __bf16 bf16x2 __attribute__((ext_vector_type(2)));
typedef short s16x4 __attribute__((ext_vector_type(4)));
typedef float f32x16 __attribute__((ext_vector_type(16)));
typedef float f32x2 __attribute__((ext_vector_type(2)));
#define DI __device__ __forceinline__
namespace gqa {
constexpr int kSq = 288, kSk = 512, kHq = 32, kHkv = 32, kB = 8;
constexpr float kC = 0.088388347648318440f * 1.4426950408889634f;

DI unsigned cvtpk(float lo, float hi) { f32x2 v = {lo, hi}; bf16x2 b = __builtin_convertvector(v, bf16x2); return __builtin_bit_cast(unsigned, b); }
DI float swapmax(float x) { auto r = __builtin_amdgcn_permlane32_swap(__float_as_uint(x), __float_as_uint(x), false, false); return fmaxf(__uint_as_float(r[0]), __uint_as_float(r[1])); }
DI float swapadd(float x) { auto r = __builtin_amdgcn_permlane32_swap(__float_as_uint(x), __float_as_uint(x), false, false); return __uint_as_float(r[0]) + __uint_as_float(r[1]); }
template <int OFF> DI s16x4 tr_read(unsigned vb) { s16x4 r; asm volatile("ds_read_b64_tr_b16 %0, %1 offset:%2\n\ts_waitcnt lgkmcnt(0)" : "=&v"(r) : "v"(vb), "i"(OFF) : "memory"); return r; }

DI void slow_block(const char* qp, const char* kp, const char* vp, char* op, int row0, char* lds, unsigned ldsa, int lane) {
  const int r32 = lane & 31, hi = lane >> 5;
  bf16x8 qf[8];
#pragma unroll
  for (int d0 = 0; d0 < 8; ++d0) qf[d0] = *(const bf16x8*)(qp + (row0 + r32) * 256 + (2 * d0 + hi) * 16);
  f32x16 o[4];
#pragma unroll
  for (int db = 0; db < 4; ++db) o[db] = f32x16{};
  float m = -__builtin_inff(), l = 0.f;
  const unsigned g = lane >> 4, dsub = g & 1, qq = (lane & 15) >> 2, pp = lane & 3;
  const unsigned vr0 = ldsa + 64u * (4 * hi + qq) + 16u * ((2 * dsub + (pp >> 1)) ^ (0u + hi)) + 8u * (pp & 1);
  const unsigned vr1 = ldsa + 2048u + 64u * (4 * hi + qq) + 16u * ((2 * dsub + (pp >> 1)) ^ (2u + hi)) + 8u * (pp & 1);
#pragma unroll 1
  for (int u = 0; u < 16; ++u) {
    f32x16 s = {};
#pragma unroll
    for (int d0 = 0; d0 < 8; ++d0) {
      const bf16x8 kf = *(const bf16x8*)(kp + (32 * u + r32) * 256 + (2 * d0 + hi) * 16);
      s = __builtin_amdgcn_mfma_f32_32x32x16_bf16(kf, qf[d0], s, 0, 0, 0);
    }
#pragma unroll
    for (int i = 0; i < 8; ++i) {
      const int n = lane + 64 * i, row = n >> 4, ch = n & 15;
      const u32x4 x = *(const u32x4*)(vp + (32 * u + row) * 256 + ch * 16);
      *(u32x4*)(lds + 2048 * (row >> 3) + 512 * (ch >> 2) + 64 * (row & 7) + 16 * ((ch & 3) ^ ((row >> 2) & 3))) = x;
    }
    float mx = -__builtin_inff();
#pragma unroll
    for (int r = 0; r < 16; ++r) { s[r] *= kC; mx = fmaxf(mx, s[r]); }
    mx = swapmax(mx);
    const float mn = fmaxf(m, mx), al = __builtin_amdgcn_exp2f(m - mn);
    m = mn; l *= al;
#pragma unroll
    for (int db = 0; db < 4; ++db)
#pragma unroll
      for (int r = 0; r < 16; ++r) o[db][r] *= al;
    float nanp = 0.f;
#pragma unroll
    for (int r = 0; r < 16; ++r) { const float x = s[r]; s[r] = __builtin_amdgcn_exp2f(x - m); l += s[r]; }
    (void)nanp;
    const u32x4 p0 = {cvtpk(s[0], s[1]), cvtpk(s[2], s[3]), cvtpk(s[4], s[5]), cvtpk(s[6], s[7])};
    const u32x4 p1 = {cvtpk(s[8], s[9]), cvtpk(s[10], s[11]), cvtpk(s[12], s[13]), cvtpk(s[14], s[15])};
    const bf16x8 P0 = __builtin_bit_cast(bf16x8, p0), P1 = __builtin_bit_cast(bf16x8, p1);
    asm volatile("s_waitcnt lgkmcnt(0)" ::: "memory");
#define GQA_PV(DB) { \
      s16x4 a0 = tr_read<512 * DB>(vr0), a1 = tr_read<512 * DB>(vr1), b0 = tr_read<4096 + 512 * DB>(vr0), b1 = tr_read<4096 + 512 * DB>(vr1); \
      __builtin_amdgcn_sched_barrier(0); \
      o[DB] = __builtin_amdgcn_mfma_f32_32x32x16_bf16(__builtin_bit_cast(bf16x8, __builtin_shufflevector(a0, a1, 0, 1, 2, 3, 4, 5, 6, 7)), P0, o[DB], 0, 0, 0); \
      o[DB] = __builtin_amdgcn_mfma_f32_32x32x16_bf16(__builtin_bit_cast(bf16x8, __builtin_shufflevector(b0, b1, 0, 1, 2, 3, 4, 5, 6, 7)), P1, o[DB], 0, 0, 0); }
    GQA_PV(0) GQA_PV(1) GQA_PV(2) GQA_PV(3)
#undef GQA_PV
  }
  const float inv = 1.0f / swapadd(l);
  char* orow = op + (row0 + r32) * 256;
#pragma unroll
  for (int db = 0; db < 4; ++db)
#pragma unroll
    for (int gq = 0; gq < 4; ++gq) {
      const u32x2 x = {cvtpk(o[db][4 * gq] * inv, o[db][4 * gq + 1] * inv), cvtpk(o[db][4 * gq + 2] * inv, o[db][4 * gq + 3] * inv)};
      *(u32x2*)(orow + (32 * db + 8 * gq + 4 * hi) * 2) = x;
    }
}

__global__ __launch_bounds__(256, 1) void attn_kernel(const char* __restrict__ q, const char* __restrict__ k, const char* __restrict__ v, char* __restrict__ o
) {
  extern __shared__ __attribute__((aligned(256))) char lds[];
  const unsigned bh = blockIdx.x;
  const unsigned kvh = (bh / kHq) * kHkv + (bh % kHq) / (kHq / kHkv);
  const unsigned w = __builtin_amdgcn_readfirstlane(threadIdx.x >> 6);
  const unsigned long long qa = (unsigned long long)(q + (size_t)bh * (kSq * 256)), ka = (unsigned long long)(k + (size_t)kvh * (kSk * 256));
  const unsigned long long va = (unsigned long long)(v + (size_t)kvh * (kSk * 256)), oa = (unsigned long long)(o + (size_t)bh * (kSq * 256));
  const unsigned ldsb = (unsigned)(size_t)lds;
  unsigned fail;
  asm volatile(
    "v_mbcnt_lo_u32_b32 v255, -1, 0\n\t"
    "v_mbcnt_hi_u32_b32 v255, -1, v255\n\t"
    "s_cmp_lt_u32 %[bid], 16\n\t"
    "s_cbranch_scc0 LNOPF\n\t"
    "s_getpc_b64 s[96:97]\n\t"
    "LPC:\n\t"
    "s_and_b32 s97, s97, 0xffff\n\t"
    "s_mov_b32 s98, LEND-LPC\n\t"
    "s_mov_b32 s99, 0x20000\n\t"
    "s_lshr_b32 s94, %[bid], 3\n\t"
    "s_lshl_b32 s94, s94, 2\n\t"
    "s_add_u32 s94, s94, %[w]\n\t"
    "s_mul_i32 s94, s94, (LEND-LPC+1023)/1024*128\n\t"
    "v_lshlrev_b32 v248, 7, v255\n\t"
    "buffer_load_dword v127, v248, s[96:99], s94 offen\n\t"
    "LNOPF:\n\t"
    "s_mov_b32 s64, %[klo]\n\t"
    "s_and_b32 s65, %[khi], 0xffff\n\t"
    "s_mov_b32 s66, 131072\n\t"
    "s_mov_b32 s67, 0x20000\n\t"
    "s_lshl_b32 s83, %[w], 11\n\t"
    "s_add_u32 s80, s83, %[lds]\n\t"
    "s_and_b32 s90, %[w], 1\n\t"
    "s_lshl_b32 s90, s90, 3\n\t"
    "v_lshrrev_b32 v240, 4, v255\n\t"
    "v_and_b32 v241, 15, v255\n\t"
    "v_xor_b32 v242, v241, v240\n\t"
    "v_lshlrev_b32 v243, 8, v240\n\t"
    "v_lshl_add_u32 v244, v242, 4, v243\n\t"
    "v_xor_b32 v242, s90, v242\n\t"
    "v_lshl_add_u32 v236, v242, 4, v243\n\t"
    "v_add_u32 v236, s83, v236\n\t"
    "v_xor_b32 v237, 64, v236\n\t"
    "s_add_u32 m0, s80, 0\n\t"
    "s_mov_b32 s81, 0\n\t"
    "buffer_load_dwordx4 v236, s[64:67], s81 offen nt lds\n\t"
    "buffer_load_dwordx4 v237, s[64:67], s81 offen offset:1024 nt lds\n\t"
    "s_mov_b32 s72, %[qlo]\n\t"
    "s_and_b32 s73, %[qhi], 0xffff\n\t"
    "s_mov_b32 s74, 73728\n\t"
    "s_mov_b32 s75, 0x20000\n\t"
    "v_xor_b32 v245, 64, v244\n\t"
    "v_xor_b32 v246, 128, v244\n\t"
    "v_xor_b32 v247, 192, v244\n\t"
    "s_lshl_b32 s82, %[w], 14\n\t"
    "s_lshl_b32 s63, %[w], 13\n\t"
    "s_add_u32 s91, s63, %[lds]\n\t"
    "s_add_u32 m0, s91, 131072\n\t"
    "s_add_u32 s81, s82, 0\n\t"
    "buffer_load_dwordx4 v244, s[72:75], s81 offen offset:0 nt lds\n\t"
    "buffer_load_dwordx4 v245, s[72:75], s81 offen offset:1024 nt lds\n\t"
    "buffer_load_dwordx4 v246, s[72:75], s81 offen offset:2048 nt lds\n\t"
    "buffer_load_dwordx4 v247, s[72:75], s81 offen offset:3072 nt lds\n\t"
    "s_add_u32 m0, s91, 135168\n\t"
    "s_add_u32 s81, s82, 4096\n\t"
    "buffer_load_dwordx4 v244, s[72:75], s81 offen offset:0 nt lds\n\t"
    "buffer_load_dwordx4 v245, s[72:75], s81 offen offset:1024 nt lds\n\t"
    "buffer_load_dwordx4 v246, s[72:75], s81 offen offset:2048 nt lds\n\t"
    "buffer_load_dwordx4 v247, s[72:75], s81 offen offset:3072 nt lds\n\t"
    "s_add_u32 m0, s91, 98304\n\t"
    "s_add_u32 s81, s82, 8192\n\t"
    "buffer_load_dwordx4 v244, s[72:75], s81 offen offset:0 nt lds\n\t"
    "buffer_load_dwordx4 v245, s[72:75], s81 offen offset:1024 nt lds\n\t"
    "buffer_load_dwordx4 v246, s[72:75], s81 offen offset:2048 nt lds\n\t"
    "buffer_load_dwordx4 v247, s[72:75], s81 offen offset:3072 nt lds\n\t"
    "s_add_u32 m0, s91, 102400\n\t"
    "s_add_u32 s81, s82, 12288\n\t"
    "buffer_load_dwordx4 v244, s[72:75], s81 offen offset:0 nt lds\n\t"
    "buffer_load_dwordx4 v245, s[72:75], s81 offen offset:1024 nt lds\n\t"
    "buffer_load_dwordx4 v246, s[72:75], s81 offen offset:2048 nt lds\n\t"
    "buffer_load_dwordx4 v247, s[72:75], s81 offen offset:3072 nt lds\n\t"
    "s_mov_b32 s68, %[vlo]\n\t"
    "s_and_b32 s69, %[vhi], 0xffff\n\t"
    "s_mov_b32 s70, 131072\n\t"
    "s_mov_b32 s71, 0x20000\n\t"
    "s_mov_b32 s76, %[olo]\n\t"
    "s_and_b32 s77, %[ohi], 0xffff\n\t"
    "s_mov_b32 s78, 73728\n\t"
    "s_mov_b32 s79, 0x20000\n\t"
    "v_and_b32 v242, 1, v240\n\t"
    "v_lshlrev_b32 v242, 3, v242\n\t"
    "v_and_b32 v248, 2, v240\n\t"
    "v_or_b32 v242, v242, v248\n\t"
    "v_xor_b32 v242, v242, v241\n\t"
    "v_lshl_add_u32 v238, v242, 4, v243\n\t"
    "v_add_u32 v238, s83, v238\n\t"
    "v_xor_b32 v239, 64, v238\n\t"
    "v_xor_b32 v242, v240, v241\n\t"
    "v_lshlrev_b32 v248, 8, v241\n\t"
    "v_lshl_add_u32 v248, v242, 4, v248\n\t"
    "v_add_u32 v224, %[lds], v248\n\t"
    "v_xor_b32 v225, 64, v224\n\t"
    "v_xor_b32 v226, 128, v224\n\t"
    "v_xor_b32 v227, 192, v224\n\t"
    "v_bfe_u32 v242, v255, 2, 2\n\t"
    "v_lshl_add_u32 v248, v240, 2, v242\n\t"
    "v_bfe_u32 v249, v248, 1, 2\n\t"
    "v_lshlrev_b32 v249, 1, v249\n\t"
    "v_and_b32 v250, 1, v248\n\t"
    "v_lshl_add_u32 v249, v250, 3, v249\n\t"
    "v_bfe_u32 v250, v255, 1, 1\n\t"
    "v_xor_b32 v249, v249, v250\n\t"
    "v_lshlrev_b32 v248, 8, v248\n\t"
    "v_lshl_add_u32 v248, v249, 4, v248\n\t"
    "v_and_b32 v250, 1, v255\n\t"
    "v_lshl_add_u32 v248, v250, 3, v248\n\t"
    "s_add_u32 s83, %[lds], 65536\n\t"
    "v_add_u32 v228, s83, v248\n\t"
    "v_xor_b32 v229, 32, v228\n\t"
    "v_xor_b32 v230, 64, v228\n\t"
    "v_xor_b32 v231, 96, v228\n\t"
    "v_xor_b32 v232, 128, v228\n\t"
    "v_xor_b32 v233, 160, v228\n\t"
    "v_xor_b32 v234, 192, v228\n\t"
    "v_xor_b32 v235, 224, v228\n\t"
    "s_mov_b32 s84, 0x3e0293ee\n\t"
    "s_mov_b32 s85, 0x3e0293ee\n\t"
    "s_mov_b32 s86, 0xffff0000\n\t"
    "s_mov_b32 s87, 0x5d800000\n\t"
    "s_mov_b32 s83, 0x3f803f80\n\t"
    "v_mov_b32 v196, s83\n\t"
    "v_mov_b32 v197, s83\n\t"
    "v_mov_b32 v198, s83\n\t"
    "v_mov_b32 v199, s83\n\t"
    "s_mov_b64 s[88:89], 0\n\t"
    "s_add_u32 s83, s63, 131072\n\t"
    "v_add_u32 v248, s83, v224\n\t"
    "v_add_u32 v249, s83, v225\n\t"
    "v_add_u32 v250, s83, v226\n\t"
    "v_add_u32 v251, s83, v227\n\t"
    "s_waitcnt vmcnt(16)\n\t"
    "s_barrier\n\t"
    "ds_read_b128 a[160:163], v224 offset:0\n\t"
    "ds_read_b128 a[164:167], v225 offset:0\n\t"
    "ds_read_b128 a[168:171], v226 offset:0\n\t"
    "ds_read_b128 a[172:175], v227 offset:0\n\t"
    "ds_read_b128 a[176:179], v224 offset:4096\n\t"
    "ds_read_b128 a[180:183], v225 offset:4096\n\t"
    "ds_read_b128 a[184:187], v226 offset:4096\n\t"
    "ds_read_b128 a[188:191], v227 offset:4096\n\t"
    "s_waitcnt vmcnt(12)\n\t"
    "ds_read_b128 v[0:3], v248 offset:0\n\t"
    "ds_read_b128 v[4:7], v249 offset:0\n\t"
    "ds_read_b128 v[8:11], v250 offset:0\n\t"
    "ds_read_b128 v[12:15], v251 offset:0\n\t"
    "s_waitcnt lgkmcnt(0)\n\t"
    "v_lshlrev_b32 v240, 16, v0\n\t"
    "v_lshlrev_b32 v242, 16, v1\n\t"
    "v_lshlrev_b32 v244, 16, v2\n\t"
    "v_lshlrev_b32 v246, 16, v3\n\t"
    "v_and_b32 v241, s86, v0\n\t"
    "v_and_b32 v243, s86, v1\n\t"
    "v_and_b32 v245, s86, v2\n\t"
    "v_and_b32 v247, s86, v3\n\t"
    "v_pk_mul_f32 v[240:241], v[240:241], s[84:85]\n\t"
    "v_pk_mul_f32 v[242:243], v[242:243], s[84:85]\n\t"
    "v_pk_mul_f32 v[244:245], v[244:245], s[84:85]\n\t"
    "v_pk_mul_f32 v[246:247], v[246:247], s[84:85]\n\t"
    "v_cvt_pk_bf16_f32 v0, v240, v241\n\t"
    "v_cvt_pk_bf16_f32 v1, v242, v243\n\t"
    "v_cvt_pk_bf16_f32 v2, v244, v245\n\t"
    "v_cvt_pk_bf16_f32 v3, v246, v247\n\t"
    "v_accvgpr_write_b32 a64, v0\n\t"
    "v_accvgpr_write_b32 a65, v1\n\t"
    "v_accvgpr_write_b32 a66, v2\n\t"
    "v_accvgpr_write_b32 a67, v3\n\t"
    "v_lshlrev_b32 v240, 16, v4\n\t"
    "v_lshlrev_b32 v242, 16, v5\n\t"
    "v_lshlrev_b32 v244, 16, v6\n\t"
    "v_lshlrev_b32 v246, 16, v7\n\t"
    "v_and_b32 v241, s86, v4\n\t"
    "v_and_b32 v243, s86, v5\n\t"
    "v_and_b32 v245, s86, v6\n\t"
    "v_and_b32 v247, s86, v7\n\t"
    "v_pk_mul_f32 v[240:241], v[240:241], s[84:85]\n\t"
    "v_pk_mul_f32 v[242:243], v[242:243], s[84:85]\n\t"
    "v_pk_mul_f32 v[244:245], v[244:245], s[84:85]\n\t"
    "v_pk_mul_f32 v[246:247], v[246:247], s[84:85]\n\t"
    "v_cvt_pk_bf16_f32 v4, v240, v241\n\t"
    "v_cvt_pk_bf16_f32 v5, v242, v243\n\t"
    "v_cvt_pk_bf16_f32 v6, v244, v245\n\t"
    "v_cvt_pk_bf16_f32 v7, v246, v247\n\t"
    "v_accvgpr_write_b32 a68, v4\n\t"
    "v_accvgpr_write_b32 a69, v5\n\t"
    "v_accvgpr_write_b32 a70, v6\n\t"
    "v_accvgpr_write_b32 a71, v7\n\t"
    "v_lshlrev_b32 v240, 16, v8\n\t"
    "v_lshlrev_b32 v242, 16, v9\n\t"
    "v_lshlrev_b32 v244, 16, v10\n\t"
    "v_lshlrev_b32 v246, 16, v11\n\t"
    "v_and_b32 v241, s86, v8\n\t"
    "v_and_b32 v243, s86, v9\n\t"
    "v_and_b32 v245, s86, v10\n\t"
    "v_and_b32 v247, s86, v11\n\t"
    "v_pk_mul_f32 v[240:241], v[240:241], s[84:85]\n\t"
    "v_pk_mul_f32 v[242:243], v[242:243], s[84:85]\n\t"
    "v_pk_mul_f32 v[244:245], v[244:245], s[84:85]\n\t"
    "v_pk_mul_f32 v[246:247], v[246:247], s[84:85]\n\t"
    "v_cvt_pk_bf16_f32 v8, v240, v241\n\t"
    "v_cvt_pk_bf16_f32 v9, v242, v243\n\t"
    "v_cvt_pk_bf16_f32 v10, v244, v245\n\t"
    "v_cvt_pk_bf16_f32 v11, v246, v247\n\t"
    "v_accvgpr_write_b32 a72, v8\n\t"
    "v_accvgpr_write_b32 a73, v9\n\t"
    "v_accvgpr_write_b32 a74, v10\n\t"
    "v_accvgpr_write_b32 a75, v11\n\t"
    "v_lshlrev_b32 v240, 16, v12\n\t"
    "v_lshlrev_b32 v242, 16, v13\n\t"
    "v_lshlrev_b32 v244, 16, v14\n\t"
    "v_lshlrev_b32 v246, 16, v15\n\t"
    "v_and_b32 v241, s86, v12\n\t"
    "v_and_b32 v243, s86, v13\n\t"
    "v_and_b32 v245, s86, v14\n\t"
    "v_and_b32 v247, s86, v15\n\t"
    "v_pk_mul_f32 v[240:241], v[240:241], s[84:85]\n\t"
    "v_pk_mul_f32 v[242:243], v[242:243], s[84:85]\n\t"
    "v_pk_mul_f32 v[244:245], v[244:245], s[84:85]\n\t"
    "v_pk_mul_f32 v[246:247], v[246:247], s[84:85]\n\t"
    "v_cvt_pk_bf16_f32 v12, v240, v241\n\t"
    "v_cvt_pk_bf16_f32 v13, v242, v243\n\t"
    "v_cvt_pk_bf16_f32 v14, v244, v245\n\t"
    "v_cvt_pk_bf16_f32 v15, v246, v247\n\t"
    "v_accvgpr_write_b32 a76, v12\n\t"
    "v_accvgpr_write_b32 a77, v13\n\t"
    "v_accvgpr_write_b32 a78, v14\n\t"
    "v_accvgpr_write_b32 a79, v15\n\t"
    "s_waitcnt vmcnt(8)\n\t"
    "ds_read_b128 v[16:19], v248 offset:4096\n\t"
    "ds_read_b128 v[20:23], v249 offset:4096\n\t"
    "ds_read_b128 v[24:27], v250 offset:4096\n\t"
    "ds_read_b128 v[28:31], v251 offset:4096\n\t"
    "s_waitcnt lgkmcnt(0)\n\t"
    "v_lshlrev_b32 v240, 16, v16\n\t"
    "v_lshlrev_b32 v242, 16, v17\n\t"
    "v_lshlrev_b32 v244, 16, v18\n\t"
    "v_lshlrev_b32 v246, 16, v19\n\t"
    "v_and_b32 v241, s86, v16\n\t"
    "v_and_b32 v243, s86, v17\n\t"
    "v_and_b32 v245, s86, v18\n\t"
    "v_and_b32 v247, s86, v19\n\t"
    "v_pk_mul_f32 v[240:241], v[240:241], s[84:85]\n\t"
    "v_pk_mul_f32 v[242:243], v[242:243], s[84:85]\n\t"
    "v_pk_mul_f32 v[244:245], v[244:245], s[84:85]\n\t"
    "v_pk_mul_f32 v[246:247], v[246:247], s[84:85]\n\t"
    "v_cvt_pk_bf16_f32 v16, v240, v241\n\t"
    "v_cvt_pk_bf16_f32 v17, v242, v243\n\t"
    "v_cvt_pk_bf16_f32 v18, v244, v245\n\t"
    "v_cvt_pk_bf16_f32 v19, v246, v247\n\t"
    "v_accvgpr_write_b32 a80, v16\n\t"
    "v_accvgpr_write_b32 a81, v17\n\t"
    "v_accvgpr_write_b32 a82, v18\n\t"
    "v_accvgpr_write_b32 a83, v19\n\t"
    "v_mfma_f32_16x16x32_bf16 v[128:131], a[160:163], a[64:67], 0\n\t"
    "v_mfma_f32_16x16x32_bf16 v[128:131], a[164:167], a[68:71], v[128:131]\n\t"
    "v_lshlrev_b32 v240, 16, v20\n\t"
    "v_lshlrev_b32 v242, 16, v21\n\t"
    "v_lshlrev_b32 v244, 16, v22\n\t"
    "v_lshlrev_b32 v246, 16, v23\n\t"
    "v_and_b32 v241, s86, v20\n\t"
    "v_and_b32 v243, s86, v21\n\t"
    "v_and_b32 v245, s86, v22\n\t"
    "v_and_b32 v247, s86, v23\n\t"
    "v_pk_mul_f32 v[240:241], v[240:241], s[84:85]\n\t"
    "v_pk_mul_f32 v[242:243], v[242:243], s[84:85]\n\t"
    "v_pk_mul_f32 v[244:245], v[244:245], s[84:85]\n\t"
    "v_pk_mul_f32 v[246:247], v[246:247], s[84:85]\n\t"
    "v_cvt_pk_bf16_f32 v20, v240, v241\n\t"
    "v_cvt_pk_bf16_f32 v21, v242, v243\n\t"
    "v_cvt_pk_bf16_f32 v22, v244, v245\n\t"
    "v_cvt_pk_bf16_f32 v23, v246, v247\n\t"
    "v_accvgpr_write_b32 a84, v20\n\t"
    "v_accvgpr_write_b32 a85, v21\n\t"
    "v_accvgpr_write_b32 a86, v22\n\t"
    "v_accvgpr_write_b32 a87, v23\n\t"
    "v_mfma_f32_16x16x32_bf16 v[128:131], a[168:171], a[72:75], v[128:131]\n\t"
    "v_mfma_f32_16x16x32_bf16 v[128:131], a[172:175], a[76:79], v[128:131]\n\t"
    "v_lshlrev_b32 v240, 16, v24\n\t"
    "v_lshlrev_b32 v242, 16, v25\n\t"
    "v_lshlrev_b32 v244, 16, v26\n\t"
    "v_lshlrev_b32 v246, 16, v27\n\t"
    "v_and_b32 v241, s86, v24\n\t"
    "v_and_b32 v243, s86, v25\n\t"
    "v_and_b32 v245, s86, v26\n\t"
    "v_and_b32 v247, s86, v27\n\t"
    "v_pk_mul_f32 v[240:241], v[240:241], s[84:85]\n\t"
    "v_pk_mul_f32 v[242:243], v[242:243], s[84:85]\n\t"
    "v_pk_mul_f32 v[244:245], v[244:245], s[84:85]\n\t"
    "v_pk_mul_f32 v[246:247], v[246:247], s[84:85]\n\t"
    "v_cvt_pk_bf16_f32 v24, v240, v241\n\t"
    "v_cvt_pk_bf16_f32 v25, v242, v243\n\t"
    "v_cvt_pk_bf16_f32 v26, v244, v245\n\t"
    "v_cvt_pk_bf16_f32 v27, v246, v247\n\t"
    "v_accvgpr_write_b32 a88, v24\n\t"
    "v_accvgpr_write_b32 a89, v25\n\t"
    "v_accvgpr_write_b32 a90, v26\n\t"
    "v_accvgpr_write_b32 a91, v27\n\t"
    "v_mfma_f32_16x16x32_bf16 v[132:135], a[176:179], a[64:67], 0\n\t"
    "v_mfma_f32_16x16x32_bf16 v[132:135], a[180:183], a[68:71], v[132:135]\n\t"
    "v_lshlrev_b32 v240, 16, v28\n\t"
    "v_lshlrev_b32 v242, 16, v29\n\t"
    "v_lshlrev_b32 v244, 16, v30\n\t"
    "v_lshlrev_b32 v246, 16, v31\n\t"
    "v_and_b32 v241, s86, v28\n\t"
    "v_and_b32 v243, s86, v29\n\t"
    "v_and_b32 v245, s86, v30\n\t"
    "v_and_b32 v247, s86, v31\n\t"
    "v_pk_mul_f32 v[240:241], v[240:241], s[84:85]\n\t"
    "v_pk_mul_f32 v[242:243], v[242:243], s[84:85]\n\t"
    "v_pk_mul_f32 v[244:245], v[244:245], s[84:85]\n\t"
    "v_pk_mul_f32 v[246:247], v[246:247], s[84:85]\n\t"
    "v_cvt_pk_bf16_f32 v28, v240, v241\n\t"
    "v_cvt_pk_bf16_f32 v29, v242, v243\n\t"
    "v_cvt_pk_bf16_f32 v30, v244, v245\n\t"
    "v_cvt_pk_bf16_f32 v31, v246, v247\n\t"
    "v_accvgpr_write_b32 a92, v28\n\t"
    "v_accvgpr_write_b32 a93, v29\n\t"
    "v_accvgpr_write_b32 a94, v30\n\t"
    "v_accvgpr_write_b32 a95, v31\n\t"
    "v_mfma_f32_16x16x32_bf16 v[132:135], a[184:187], a[72:75], v[132:135]\n\t"
    "v_mfma_f32_16x16x32_bf16 v[132:135], a[188:191], a[76:79], v[132:135]\n\t"
    "s_nop 1\n\t"
    "v_mfma_f32_16x16x32_bf16 v[136:139], a[160:163], a[80:83], 0\n\t"
    "v_mfma_f32_16x16x32_bf16 v[136:139], a[164:167], a[84:87], v[136:139]\n\t"
    "v_mfma_f32_16x16x32_bf16 v[136:139], a[168:171], a[88:91], v[136:139]\n\t"
    "v_mfma_f32_16x16x32_bf16 v[136:139], a[172:175], a[92:95], v[136:139]\n\t"
    "v_mfma_f32_16x16x32_bf16 v[140:143], a[176:179], a[80:83], 0\n\t"
    "v_mfma_f32_16x16x32_bf16 v[140:143], a[180:183], a[84:87], v[140:143]\n\t"
    "v_mfma_f32_16x16x32_bf16 v[140:143], a[184:187], a[88:91], v[140:143]\n\t"
    "v_mfma_f32_16x16x32_bf16 v[140:143], a[188:191], a[92:95], v[140:143]\n\t"
    "s_add_u32 s83, s63, 98304\n\t"
    "v_add_u32 v248, s83, v224\n\t"
    "v_add_u32 v249, s83, v225\n\t"
    "v_add_u32 v250, s83, v226\n\t"
    "v_add_u32 v251, s83, v227\n\t"
    "s_nop 1\n\t"
    "v_max3_f32 v240, v128, v129, v130\n\t"
    "v_max3_f32 v242, v136, v137, v138\n\t"
    "v_max3_f32 v241, v131, v132, v133\n\t"
    "v_max3_f32 v243, v139, v140, v141\n\t"
    "v_max3_f32 v240, v240, v134, v135\n\t"
    "v_max3_f32 v242, v242, v142, v143\n\t"
    "v_max_f32 v240, v240, v241\n\t"
    "v_max_f32 v242, v242, v243\n\t"
    "v_mov_b32 v241, v240\n\t"
    "v_mov_b32 v243, v242\n\t"
    "s_nop 1\n\t"
    "v_permlane32_swap_b32 v240, v241\n\t"
    "v_permlane32_swap_b32 v242, v243\n\t"
    "v_max_f32 v240, v240, v241\n\t"
    "v_max_f32 v242, v242, v243\n\t"
    "v_mov_b32 v241, v240\n\t"
    "v_mov_b32 v243, v242\n\t"
    "s_nop 1\n\t"
    "v_permlane16_swap_b32 v240, v241\n\t"
    "v_permlane16_swap_b32 v242, v243\n\t"
    "v_min_f32 v240, -v240, -v241\n\t"
    "v_min_f32 v242, -v242, -v243\n\t"
    "v_mov_b32 v176, v240\n\t"
    "v_mov_b32 v177, v240\n\t"
    "v_mov_b32 v178, v240\n\t"
    "v_mov_b32 v179, v240\n\t"
    "v_mov_b32 v180, v242\n\t"
    "v_mov_b32 v181, v242\n\t"
    "v_mov_b32 v182, v242\n\t"
    "v_mov_b32 v183, v242\n\t"
    "v_add_f32 v128, v240, v128\n\t"
    "v_add_f32 v129, v240, v129\n\t"
    "v_add_f32 v130, v240, v130\n\t"
    "v_add_f32 v131, v240, v131\n\t"
    "v_add_f32 v132, v240, v132\n\t"
    "v_add_f32 v133, v240, v133\n\t"
    "v_add_f32 v134, v240, v134\n\t"
    "v_add_f32 v135, v240, v135\n\t"
    "v_add_f32 v136, v242, v136\n\t"
    "v_add_f32 v137, v242, v137\n\t"
    "v_add_f32 v138, v242, v138\n\t"
    "v_add_f32 v139, v242, v139\n\t"
    "v_add_f32 v140, v242, v140\n\t"
    "v_add_f32 v141, v242, v141\n\t"
    "v_add_f32 v142, v242, v142\n\t"
    "v_add_f32 v143, v242, v143\n\t"
    "v_exp_f32 v128, v128\n\t"
    "v_exp_f32 v129, v129\n\t"
    "v_exp_f32 v130, v130\n\t"
    "v_exp_f32 v131, v131\n\t"
    "v_exp_f32 v132, v132\n\t"
    "v_exp_f32 v133, v133\n\t"
    "v_exp_f32 v134, v134\n\t"
    "v_exp_f32 v135, v135\n\t"
    "v_exp_f32 v136, v136\n\t"
    "v_exp_f32 v137, v137\n\t"
    "v_exp_f32 v138, v138\n\t"
    "v_exp_f32 v139, v139\n\t"
    "v_exp_f32 v140, v140\n\t"
    "v_exp_f32 v141, v141\n\t"
    "v_exp_f32 v142, v142\n\t"
    "v_exp_f32 v143, v143\n\t"
    "v_cvt_pk_bf16_f32 v200, v128, v129\n\t"
    "v_cvt_pk_bf16_f32 v201, v130, v131\n\t"
    "v_cvt_pk_bf16_f32 v202, v132, v133\n\t"
    "v_cvt_pk_bf16_f32 v203, v134, v135\n\t"
    "v_cvt_pk_bf16_f32 v204, v136, v137\n\t"
    "v_cvt_pk_bf16_f32 v205, v138, v139\n\t"
    "v_cvt_pk_bf16_f32 v206, v140, v141\n\t"
    "v_cvt_pk_bf16_f32 v207, v142, v143\n\t"
    "s_add_u32 m0, s80, 8192\n\t"
    "s_mov_b32 s81, 8192\n\t"
    "buffer_load_dwordx4 v236, s[64:67], s81 offen nt lds\n\t"
    "buffer_load_dwordx4 v237, s[64:67], s81 offen offset:1024 nt lds\n\t"
    "s_waitcnt vmcnt(6)\n\t"
    "ds_read_b128 v[0:3], v248 offset:0\n\t"
    "ds_read_b128 v[4:7], v249 offset:0\n\t"
    "ds_read_b128 v[8:11], v250 offset:0\n\t"
    "ds_read_b128 v[12:15], v251 offset:0\n\t"
    "s_waitcnt lgkmcnt(0)\n\t"
    "v_lshlrev_b32 v240, 16, v0\n\t"
    "v_lshlrev_b32 v242, 16, v1\n\t"
    "v_lshlrev_b32 v244, 16, v2\n\t"
    "v_lshlrev_b32 v246, 16, v3\n\t"
    "v_and_b32 v241, s86, v0\n\t"
    "v_and_b32 v243, s86, v1\n\t"
    "v_and_b32 v245, s86, v2\n\t"
    "v_and_b32 v247, s86, v3\n\t"
    "v_pk_mul_f32 v[240:241], v[240:241], s[84:85]\n\t"
    "v_pk_mul_f32 v[242:243], v[242:243], s[84:85]\n\t"
    "v_pk_mul_f32 v[244:245], v[244:245], s[84:85]\n\t"
    "v_pk_mul_f32 v[246:247], v[246:247], s[84:85]\n\t"
    "v_cvt_pk_bf16_f32 v0, v240, v241\n\t"
    "v_cvt_pk_bf16_f32 v1, v242, v243\n\t"
    "v_cvt_pk_bf16_f32 v2, v244, v245\n\t"
    "v_cvt_pk_bf16_f32 v3, v246, v247\n\t"
    "v_accvgpr_write_b32 a96, v0\n\t"
    "v_accvgpr_write_b32 a97, v1\n\t"
    "v_accvgpr_write_b32 a98, v2\n\t"
    "v_accvgpr_write_b32 a99, v3\n\t"
    "v_lshlrev_b32 v240, 16, v4\n\t"
    "v_lshlrev_b32 v242, 16, v5\n\t"
    "v_lshlrev_b32 v244, 16, v6\n\t"
    "v_lshlrev_b32 v246, 16, v7\n\t"
    "v_and_b32 v241, s86, v4\n\t"
    "v_and_b32 v243, s86, v5\n\t"
    "v_and_b32 v245, s86, v6\n\t"
    "v_and_b32 v247, s86, v7\n\t"
    "v_pk_mul_f32 v[240:241], v[240:241], s[84:85]\n\t"
    "v_pk_mul_f32 v[242:243], v[242:243], s[84:85]\n\t"
    "v_pk_mul_f32 v[244:245], v[244:245], s[84:85]\n\t"
    "v_pk_mul_f32 v[246:247], v[246:247], s[84:85]\n\t"
    "v_cvt_pk_bf16_f32 v4, v240, v241\n\t"
    "v_cvt_pk_bf16_f32 v5, v242, v243\n\t"
    "v_cvt_pk_bf16_f32 v6, v244, v245\n\t"
    "v_cvt_pk_bf16_f32 v7, v246, v247\n\t"
    "v_accvgpr_write_b32 a100, v4\n\t"
    "v_accvgpr_write_b32 a101, v5\n\t"
    "v_accvgpr_write_b32 a102, v6\n\t"
    "v_accvgpr_write_b32 a103, v7\n\t"
    "s_add_u32 m0, s80, 65536\n\t"
    "s_mov_b32 s81, 0\n\t"
    "buffer_load_dwordx4 v238, s[68:71], s81 offen nt lds\n\t"
    "v_lshlrev_b32 v240, 16, v8\n\t"
    "v_lshlrev_b32 v242, 16, v9\n\t"
    "v_lshlrev_b32 v244, 16, v10\n\t"
    "v_lshlrev_b32 v246, 16, v11\n\t"
    "v_and_b32 v241, s86, v8\n\t"
    "v_and_b32 v243, s86, v9\n\t"
    "v_and_b32 v245, s86, v10\n\t"
    "v_and_b32 v247, s86, v11\n\t"
    "v_pk_mul_f32 v[240:241], v[240:241], s[84:85]\n\t"
    "v_pk_mul_f32 v[242:243], v[242:243], s[84:85]\n\t"
    "v_pk_mul_f32 v[244:245], v[244:245], s[84:85]\n\t"
    "v_pk_mul_f32 v[246:247], v[246:247], s[84:85]\n\t"
    "v_cvt_pk_bf16_f32 v8, v240, v241\n\t"
    "v_cvt_pk_bf16_f32 v9, v242, v243\n\t"
    "v_cvt_pk_bf16_f32 v10, v244, v245\n\t"
    "v_cvt_pk_bf16_f32 v11, v246, v247\n\t"
    "v_accvgpr_write_b32 a104, v8\n\t"
    "v_accvgpr_write_b32 a105, v9\n\t"
    "v_accvgpr_write_b32 a106, v10\n\t"
    "v_accvgpr_write_b32 a107, v11\n\t"
    "v_lshlrev_b32 v240, 16, v12\n\t"
    "v_lshlrev_b32 v242, 16, v13\n\t"
    "v_lshlrev_b32 v244, 16, v14\n\t"
    "v_lshlrev_b32 v246, 16, v15\n\t"
    "v_and_b32 v241, s86, v12\n\t"
    "v_and_b32 v243, s86, v13\n\t"
    "v_and_b32 v245, s86, v14\n\t"
    "v_and_b32 v247, s86, v15\n\t"
    "v_pk_mul_f32 v[240:241], v[240:241], s[84:85]\n\t"
    "v_pk_mul_f32 v[242:243], v[242:243], s[84:85]\n\t"
    "v_pk_mul_f32 v[244:245], v[244:245], s[84:85]\n\t"
    "v_pk_mul_f32 v[246:247], v[246:247], s[84:85]\n\t"
    "v_cvt_pk_bf16_f32 v12, v240, v241\n\t"
    "v_cvt_pk_bf16_f32 v13, v242, v243\n\t"
    "v_cvt_pk_bf16_f32 v14, v244, v245\n\t"
    "v_cvt_pk_bf16_f32 v15, v246, v247\n\t"
    "v_accvgpr_write_b32 a108, v12\n\t"
    "v_accvgpr_write_b32 a109, v13\n\t"
    "v_accvgpr_write_b32 a110, v14\n\t"
    "v_accvgpr_write_b32 a111, v15\n\t"
    "buffer_load_dwordx4 v239, s[68:71], s81 offen offset:1024 nt lds\n\t"
    "s_waitcnt vmcnt(4)\n\t"
    "ds_read_b128 v[16:19], v248 offset:4096\n\t"
    "ds_read_b128 v[20:23], v249 offset:4096\n\t"
    "ds_read_b128 v[24:27], v250 offset:4096\n\t"
    "ds_read_b128 v[28:31], v251 offset:4096\n\t"
    "s_waitcnt lgkmcnt(0)\n\t"
    "v_lshlrev_b32 v240, 16, v16\n\t"
    "v_lshlrev_b32 v242, 16, v17\n\t"
    "v_lshlrev_b32 v244, 16, v18\n\t"
    "v_lshlrev_b32 v246, 16, v19\n\t"
    "v_and_b32 v241, s86, v16\n\t"
    "v_and_b32 v243, s86, v17\n\t"
    "v_and_b32 v245, s86, v18\n\t"
    "v_and_b32 v247, s86, v19\n\t"
    "v_pk_mul_f32 v[240:241], v[240:241], s[84:85]\n\t"
    "v_pk_mul_f32 v[242:243], v[242:243], s[84:85]\n\t"
    "v_pk_mul_f32 v[244:245], v[244:245], s[84:85]\n\t"
    "v_pk_mul_f32 v[246:247], v[246:247], s[84:85]\n\t"
    "v_cvt_pk_bf16_f32 v16, v240, v241\n\t"
    "v_cvt_pk_bf16_f32 v17, v242, v243\n\t"
    "v_cvt_pk_bf16_f32 v18, v244, v245\n\t"
    "v_cvt_pk_bf16_f32 v19, v246, v247\n\t"
    "v_accvgpr_write_b32 a112, v16\n\t"
    "v_accvgpr_write_b32 a113, v17\n\t"
    "v_accvgpr_write_b32 a114, v18\n\t"
    "v_accvgpr_write_b32 a115, v19\n\t"
    "v_mfma_f32_16x16x32_bf16 v[144:147], a[160:163], a[96:99], 0\n\t"
    "v_mfma_f32_16x16x32_bf16 v[144:147], a[164:167], a[100:103], v[144:147]\n\t"
    "v_lshlrev_b32 v240, 16, v20\n\t"
    "v_lshlrev_b32 v242, 16, v21\n\t"
    "v_lshlrev_b32 v244, 16, v22\n\t"
    "v_lshlrev_b32 v246, 16, v23\n\t"
    "v_and_b32 v241, s86, v20\n\t"
    "v_and_b32 v243, s86, v21\n\t"
    "v_and_b32 v245, s86, v22\n\t"
    "v_and_b32 v247, s86, v23\n\t"
    "v_pk_mul_f32 v[240:241], v[240:241], s[84:85]\n\t"
    "v_pk_mul_f32 v[242:243], v[242:243], s[84:85]\n\t"
    "v_pk_mul_f32 v[244:245], v[244:245], s[84:85]\n\t"
    "v_pk_mul_f32 v[246:247], v[246:247], s[84:85]\n\t"
    "v_cvt_pk_bf16_f32 v20, v240, v241\n\t"
    "v_cvt_pk_bf16_f32 v21, v242, v243\n\t"
    "v_cvt_pk_bf16_f32 v22, v244, v245\n\t"
    "v_cvt_pk_bf16_f32 v23, v246, v247\n\t"
    "v_accvgpr_write_b32 a116, v20\n\t"
    "v_accvgpr_write_b32 a117, v21\n\t"
    "v_accvgpr_write_b32 a118, v22\n\t"
    "v_accvgpr_write_b32 a119, v23\n\t"
    "v_mfma_f32_16x16x32_bf16 v[144:147], a[168:171], a[104:107], v[144:147]\n\t"
    "v_mfma_f32_16x16x32_bf16 v[144:147], a[172:175], a[108:111], v[144:147]\n\t"
    "s_add_u32 m0, s80, 73728\n\t"
    "s_mov_b32 s81, 8192\n\t"
    "buffer_load_dwordx4 v238, s[68:71], s81 offen nt lds\n\t"
    "v_lshlrev_b32 v240, 16, v24\n\t"
    "v_lshlrev_b32 v242, 16, v25\n\t"
    "v_lshlrev_b32 v244, 16, v26\n\t"
    "v_lshlrev_b32 v246, 16, v27\n\t"
    "v_and_b32 v241, s86, v24\n\t"
    "v_and_b32 v243, s86, v25\n\t"
    "v_and_b32 v245, s86, v26\n\t"
    "v_and_b32 v247, s86, v27\n\t"
    "v_pk_mul_f32 v[240:241], v[240:241], s[84:85]\n\t"
    "v_pk_mul_f32 v[242:243], v[242:243], s[84:85]\n\t"
    "v_pk_mul_f32 v[244:245], v[244:245], s[84:85]\n\t"
    "v_pk_mul_f32 v[246:247], v[246:247], s[84:85]\n\t"
    "v_cvt_pk_bf16_f32 v24, v240, v241\n\t"
    "v_cvt_pk_bf16_f32 v25, v242, v243\n\t"
    "v_cvt_pk_bf16_f32 v26, v244, v245\n\t"
    "v_cvt_pk_bf16_f32 v27, v246, v247\n\t"
    "v_accvgpr_write_b32 a120, v24\n\t"
    "v_accvgpr_write_b32 a121, v25\n\t"
    "v_accvgpr_write_b32 a122, v26\n\t"
    "v_accvgpr_write_b32 a123, v27\n\t"
    "v_mfma_f32_16x16x32_bf16 v[148:151], a[176:179], a[96:99], 0\n\t"
    "v_mfma_f32_16x16x32_bf16 v[148:151], a[180:183], a[100:103], v[148:151]\n\t"
    "v_lshlrev_b32 v240, 16, v28\n\t"
    "v_lshlrev_b32 v242, 16, v29\n\t"
    "v_lshlrev_b32 v244, 16, v30\n\t"
    "v_lshlrev_b32 v246, 16, v31\n\t"
    "v_and_b32 v241, s86, v28\n\t"
    "v_and_b32 v243, s86, v29\n\t"
    "v_and_b32 v245, s86, v30\n\t"
    "v_and_b32 v247, s86, v31\n\t"
    "v_pk_mul_f32 v[240:241], v[240:241], s[84:85]\n\t"
    "v_pk_mul_f32 v[242:243], v[242:243], s[84:85]\n\t"
    "v_pk_mul_f32 v[244:245], v[244:245], s[84:85]\n\t"
    "v_pk_mul_f32 v[246:247], v[246:247], s[84:85]\n\t"
    "v_cvt_pk_bf16_f32 v28, v240, v241\n\t"
    "v_cvt_pk_bf16_f32 v29, v242, v243\n\t"
    "v_cvt_pk_bf16_f32 v30, v244, v245\n\t"
    "v_cvt_pk_bf16_f32 v31, v246, v247\n\t"
    "v_accvgpr_write_b32 a124, v28\n\t"
    "v_accvgpr_write_b32 a125, v29\n\t"
    "v_accvgpr_write_b32 a126, v30\n\t"
    "v_accvgpr_write_b32 a127, v31\n\t"
    "v_mfma_f32_16x16x32_bf16 v[148:151], a[184:187], a[104:107], v[148:151]\n\t"
    "v_mfma_f32_16x16x32_bf16 v[148:151], a[188:191], a[108:111], v[148:151]\n\t"
    "buffer_load_dwordx4 v239, s[68:71], s81 offen offset:1024 nt lds\n\t"
    "s_nop 1\n\t"
    "v_mfma_f32_16x16x32_bf16 v[152:155], a[160:163], a[112:115], 0\n\t"
    "v_mfma_f32_16x16x32_bf16 v[152:155], a[164:167], a[116:119], v[152:155]\n\t"
    "v_mfma_f32_16x16x32_bf16 v[152:155], a[168:171], a[120:123], v[152:155]\n\t"
    "v_mfma_f32_16x16x32_bf16 v[152:155], a[172:175], a[124:127], v[152:155]\n\t"
    "v_mfma_f32_16x16x32_bf16 v[156:159], a[176:179], a[112:115], 0\n\t"
    "v_mfma_f32_16x16x32_bf16 v[156:159], a[180:183], a[116:119], v[156:159]\n\t"
    "v_mfma_f32_16x16x32_bf16 v[156:159], a[184:187], a[120:123], v[156:159]\n\t"
    "v_mfma_f32_16x16x32_bf16 v[156:159], a[188:191], a[124:127], v[156:159]\n\t"
    "s_add_u32 m0, s80, 16384\n\t"
    "s_mov_b32 s81, 16384\n\t"
    "buffer_load_dwordx4 v236, s[64:67], s81 offen nt lds\n\t"
    "buffer_load_dwordx4 v237, s[64:67], s81 offen offset:1024 nt lds\n\t"
    "s_add_u32 m0, s80, 57344\n\t"
    "s_mov_b32 s81, 65536\n\t"
    "buffer_load_dwordx4 v236, s[72:75], s81 offen nt lds\n\t"
    "buffer_load_dwordx4 v237, s[72:75], s81 offen offset:1024 nt lds\n\t"
    "s_waitcnt vmcnt(6)\n\t"
    "s_barrier\n\t"
    "ds_read_b128 a[160:163], v224 offset:8192\n\t"
    "ds_read_b128 a[164:167], v225 offset:8192\n\t"
    "ds_read_b128 a[168:171], v226 offset:8192\n\t"
    "ds_read_b128 a[172:175], v227 offset:8192\n\t"
    "ds_read_b128 a[176:179], v224 offset:12288\n\t"
    "ds_read_b128 a[180:183], v225 offset:12288\n\t"
    "ds_read_b128 a[184:187], v226 offset:12288\n\t"
    "ds_read_b128 a[188:191], v227 offset:12288\n\t"
    "v_max3_f32 v240, v144, v145, v146\n\t"
    "v_max3_f32 v242, v152, v153, v154\n\t"
    "v_max3_f32 v241, v147, v148, v149\n\t"
    "v_max3_f32 v243, v155, v156, v157\n\t"
    "v_max3_f32 v240, v240, v150, v151\n\t"
    "v_max3_f32 v242, v242, v158, v159\n\t"
    "v_max_f32 v240, v240, v241\n\t"
    "v_max_f32 v242, v242, v243\n\t"
    "v_mov_b32 v241, v240\n\t"
    "v_mov_b32 v243, v242\n\t"
    "s_nop 1\n\t"
    "v_permlane32_swap_b32 v240, v241\n\t"
    "v_permlane32_swap_b32 v242, v243\n\t"
    "v_max_f32 v240, v240, v241\n\t"
    "v_max_f32 v242, v242, v243\n\t"
    "v_mov_b32 v241, v240\n\t"
    "v_mov_b32 v243, v242\n\t"
    "s_nop 1\n\t"
    "v_permlane16_swap_b32 v240, v241\n\t"
    "v_permlane16_swap_b32 v242, v243\n\t"
    "v_min_f32 v240, -v240, -v241\n\t"
    "v_min_f32 v242, -v242, -v243\n\t"
    "v_mov_b32 v184, v240\n\t"
    "v_mov_b32 v185, v240\n\t"
    "v_mov_b32 v186, v240\n\t"
    "v_mov_b32 v187, v240\n\t"
    "v_mov_b32 v188, v242\n\t"
    "v_mov_b32 v189, v242\n\t"
    "v_mov_b32 v190, v242\n\t"
    "v_mov_b32 v191, v242\n\t"
    "v_add_f32 v144, v240, v144\n\t"
    "v_add_f32 v145, v240, v145\n\t"
    "v_add_f32 v146, v240, v146\n\t"
    "v_add_f32 v147, v240, v147\n\t"
    "v_add_f32 v148, v240, v148\n\t"
    "v_add_f32 v149, v240, v149\n\t"
    "v_add_f32 v150, v240, v150\n\t"
    "v_add_f32 v151, v240, v151\n\t"
    "v_add_f32 v152, v242, v152\n\t"
    "v_add_f32 v153, v242, v153\n\t"
    "v_add_f32 v154, v242, v154\n\t"
    "v_add_f32 v155, v242, v155\n\t"
    "v_add_f32 v156, v242, v156\n\t"
    "v_add_f32 v157, v242, v157\n\t"
    "v_add_f32 v158, v242, v158\n\t"
    "v_add_f32 v159, v242, v159\n\t"
    "ds_read_b64_tr_b16 a[192:193], v228 offset:0\n\t"
    "ds_read_b64_tr_b16 a[194:195], v228 offset:4096\n\t"
    "ds_read_b64_tr_b16 a[196:197], v229 offset:0\n\t"
    "ds_read_b64_tr_b16 a[198:199], v229 offset:4096\n\t"
    "ds_read_b64_tr_b16 a[200:201], v230 offset:0\n\t"
    "ds_read_b64_tr_b16 a[202:203], v230 offset:4096\n\t"
    "ds_read_b64_tr_b16 a[204:205], v231 offset:0\n\t"
    "ds_read_b64_tr_b16 a[206:207], v231 offset:4096\n\t"
    "ds_read_b64_tr_b16 a[208:209], v232 offset:0\n\t"
    "ds_read_b64_tr_b16 a[210:211], v232 offset:4096\n\t"
    "ds_read_b64_tr_b16 a[212:213], v233 offset:0\n\t"
    "ds_read_b64_tr_b16 a[214:215], v233 offset:4096\n\t"
    "ds_read_b64_tr_b16 a[216:217], v234 offset:0\n\t"
    "ds_read_b64_tr_b16 a[218:219], v234 offset:4096\n\t"
    "ds_read_b64_tr_b16 a[220:221], v235 offset:0\n\t"
    "ds_read_b64_tr_b16 a[222:223], v235 offset:4096\n\t"
    "s_waitcnt lgkmcnt(15)\n\t"
    "v_mfma_f32_16x16x32_bf16 v[128:131], a[160:163], a[64:67], v[176:179]\n\t"
    "v_exp_f32 v144, v144\n\t"
    "v_mfma_f32_16x16x32_bf16 v[128:131], a[164:167], a[68:71], v[128:131]\n\t"
    "v_mfma_f32_16x16x32_bf16 v[128:131], a[168:171], a[72:75], v[128:131]\n\t"
    "v_exp_f32 v145, v145\n\t"
    "v_mfma_f32_16x16x32_bf16 v[128:131], a[172:175], a[76:79], v[128:131]\n\t"
    "v_cvt_pk_bf16_f32 v208, v144, v145\n\t"
    "v_mfma_f32_16x16x32_bf16 v[132:135], a[176:179], a[64:67], v[176:179]\n\t"
    "v_exp_f32 v146, v146\n\t"
    "v_mfma_f32_16x16x32_bf16 v[132:135], a[180:183], a[68:71], v[132:135]\n\t"
    "v_mfma_f32_16x16x32_bf16 v[132:135], a[184:187], a[72:75], v[132:135]\n\t"
    "v_exp_f32 v147, v147\n\t"
    "v_mfma_f32_16x16x32_bf16 v[132:135], a[188:191], a[76:79], v[132:135]\n\t"
    "v_cvt_pk_bf16_f32 v209, v146, v147\n\t"
    "v_mfma_f32_16x16x32_bf16 v[136:139], a[160:163], a[80:83], v[180:183]\n\t"
    "v_exp_f32 v148, v148\n\t"
    "v_mfma_f32_16x16x32_bf16 v[136:139], a[164:167], a[84:87], v[136:139]\n\t"
    "v_mfma_f32_16x16x32_bf16 v[136:139], a[168:171], a[88:91], v[136:139]\n\t"
    "v_exp_f32 v149, v149\n\t"
    "v_mfma_f32_16x16x32_bf16 v[136:139], a[172:175], a[92:95], v[136:139]\n\t"
    "v_cvt_pk_bf16_f32 v210, v148, v149\n\t"
    "v_mfma_f32_16x16x32_bf16 v[140:143], a[176:179], a[80:83], v[180:183]\n\t"
    "v_exp_f32 v150, v150\n\t"
    "v_mfma_f32_16x16x32_bf16 v[140:143], a[180:183], a[84:87], v[140:143]\n\t"
    "v_mfma_f32_16x16x32_bf16 v[140:143], a[184:187], a[88:91], v[140:143]\n\t"
    "v_exp_f32 v151, v151\n\t"
    "v_mfma_f32_16x16x32_bf16 v[140:143], a[188:191], a[92:95], v[140:143]\n\t"
    "v_cvt_pk_bf16_f32 v211, v150, v151\n\t"
    "s_waitcnt lgkmcnt(0)\n\t"
    "v_mfma_f32_16x16x32_bf16 v[0:3], a[192:195], v[200:203], 0\n\t"
    "v_exp_f32 v152, v152\n\t"
    "v_mfma_f32_16x16x32_bf16 v[32:35], a[192:195], v[204:207], 0\n\t"
    "v_mfma_f32_16x16x32_bf16 v[4:7], a[196:199], v[200:203], 0\n\t"
    "v_exp_f32 v153, v153\n\t"
    "v_mfma_f32_16x16x32_bf16 v[36:39], a[196:199], v[204:207], 0\n\t"
    "v_cvt_pk_bf16_f32 v212, v152, v153\n\t"
    "v_mfma_f32_16x16x32_bf16 v[8:11], a[200:203], v[200:203], 0\n\t"
    "v_exp_f32 v154, v154\n\t"
    "v_mfma_f32_16x16x32_bf16 v[40:43], a[200:203], v[204:207], 0\n\t"
    "v_mfma_f32_16x16x32_bf16 v[12:15], a[204:207], v[200:203], 0\n\t"
    "v_exp_f32 v155, v155\n\t"
    "v_mfma_f32_16x16x32_bf16 v[44:47], a[204:207], v[204:207], 0\n\t"
    "v_cvt_pk_bf16_f32 v213, v154, v155\n\t"
    "v_mfma_f32_16x16x32_bf16 v[16:19], a[208:211], v[200:203], 0\n\t"
    "v_exp_f32 v156, v156\n\t"
    "v_mfma_f32_16x16x32_bf16 v[48:51], a[208:211], v[204:207], 0\n\t"
    "v_mfma_f32_16x16x32_bf16 v[20:23], a[212:215], v[200:203], 0\n\t"
    "v_exp_f32 v157, v157\n\t"
    "v_mfma_f32_16x16x32_bf16 v[52:55], a[212:215], v[204:207], 0\n\t"
    "v_cvt_pk_bf16_f32 v214, v156, v157\n\t"
    "v_mfma_f32_16x16x32_bf16 v[24:27], a[216:219], v[200:203], 0\n\t"
    "v_exp_f32 v158, v158\n\t"
    "v_mfma_f32_16x16x32_bf16 v[56:59], a[216:219], v[204:207], 0\n\t"
    "v_mfma_f32_16x16x32_bf16 v[28:31], a[220:223], v[200:203], 0\n\t"
    "v_exp_f32 v159, v159\n\t"
    "v_mfma_f32_16x16x32_bf16 v[60:63], a[220:223], v[204:207], 0\n\t"
    "v_cvt_pk_bf16_f32 v215, v158, v159\n\t"
    "v_mfma_f32_16x16x32_bf16 a[240:243], v[196:199], v[200:203], 0\n\t"
    "v_exp_f32 v128, v128\n\t"
    "v_mfma_f32_16x16x32_bf16 a[244:247], v[196:199], v[204:207], 0\n\t"
    "s_waitcnt vmcnt(0)\n\t"
    "s_barrier\n\t"
    "v_mfma_f32_16x16x32_bf16 v[144:147], a[160:163], a[96:99], v[184:187]\n\t"
    "v_exp_f32 v129, v129\n\t"
    "v_mfma_f32_16x16x32_bf16 v[144:147], a[164:167], a[100:103], v[144:147]\n\t"
    "ds_read_b128 a[128:131], v224 offset:57344\n\t"
    "v_cvt_pk_bf16_f32 v200, v128, v129\n\t"
    "s_add_u32 m0, s80, 81920\n\t"
    "v_mfma_f32_16x16x32_bf16 v[144:147], a[168:171], a[104:107], v[144:147]\n\t"
    "v_exp_f32 v130, v130\n\t"
    "v_mfma_f32_16x16x32_bf16 v[144:147], a[172:175], a[108:111], v[144:147]\n\t"
    "ds_read_b128 a[132:135], v225 offset:57344\n\t"
    "s_mov_b32 s81, 16384\n\t"
    "v_mfma_f32_16x16x32_bf16 v[148:151], a[176:179], a[96:99], v[184:187]\n\t"
    "v_exp_f32 v131, v131\n\t"
    "v_mfma_f32_16x16x32_bf16 v[148:151], a[180:183], a[100:103], v[148:151]\n\t"
    "ds_read_b128 a[136:139], v226 offset:57344\n\t"
    "v_cvt_pk_bf16_f32 v201, v130, v131\n\t"
    "v_mfma_f32_16x16x32_bf16 v[148:151], a[184:187], a[104:107], v[148:151]\n\t"
    "v_exp_f32 v132, v132\n\t"
    "v_mfma_f32_16x16x32_bf16 v[148:151], a[188:191], a[108:111], v[148:151]\n\t"
    "ds_read_b128 a[140:143], v227 offset:57344\n\t"
    "buffer_load_dwordx4 v238, s[68:71], s81 offen nt lds\n\t"
    "v_mfma_f32_16x16x32_bf16 v[152:155], a[160:163], a[112:115], v[188:191]\n\t"
    "v_exp_f32 v133, v133\n\t"
    "v_mfma_f32_16x16x32_bf16 v[152:155], a[164:167], a[116:119], v[152:155]\n\t"
    "ds_read_b128 a[144:147], v224 offset:61440\n\t"
    "v_cvt_pk_bf16_f32 v202, v132, v133\n\t"
    "v_mfma_f32_16x16x32_bf16 v[152:155], a[168:171], a[120:123], v[152:155]\n\t"
    "v_exp_f32 v134, v134\n\t"
    "v_mfma_f32_16x16x32_bf16 v[152:155], a[172:175], a[124:127], v[152:155]\n\t"
    "ds_read_b128 a[160:163], v224 offset:16384\n\t"
    "v_mfma_f32_16x16x32_bf16 v[156:159], a[176:179], a[112:115], v[188:191]\n\t"
    "v_exp_f32 v135, v135\n\t"
    "v_mfma_f32_16x16x32_bf16 v[156:159], a[180:183], a[116:119], v[156:159]\n\t"
    "ds_read_b128 a[148:151], v225 offset:61440\n\t"
    "v_cvt_pk_bf16_f32 v203, v134, v135\n\t"
    "v_mfma_f32_16x16x32_bf16 v[156:159], a[184:187], a[120:123], v[156:159]\n\t"
    "v_exp_f32 v136, v136\n\t"
    "v_mfma_f32_16x16x32_bf16 v[156:159], a[188:191], a[124:127], v[156:159]\n\t"
    "ds_read_b128 a[164:167], v225 offset:16384\n\t"
    "buffer_load_dwordx4 v239, s[68:71], s81 offen offset:1024 nt lds\n\t"
    "v_mfma_f32_16x16x32_bf16 v[64:67], a[192:195], v[208:211], 0\n\t"
    "ds_read_b128 a[168:171], v226 offset:16384\n\t"
    "v_exp_f32 v137, v137\n\t"
    "v_mfma_f32_16x16x32_bf16 v[96:99], a[192:195], v[212:215], 0\n\t"
    "ds_read_b128 a[152:155], v226 offset:61440\n\t"
    "v_cvt_pk_bf16_f32 v204, v136, v137\n\t"
    "v_mfma_f32_16x16x32_bf16 v[68:71], a[196:199], v[208:211], 0\n\t"
    "ds_read_b128 a[172:175], v227 offset:16384\n\t"
    "v_exp_f32 v138, v138\n\t"
    "v_mfma_f32_16x16x32_bf16 v[100:103], a[196:199], v[212:215], 0\n\t"
    "ds_read_b128 a[176:179], v224 offset:20480\n\t"
    "s_add_u32 m0, s80, 24576\n\t"
    "s_mov_b32 s81, 24576\n\t"
    "v_mfma_f32_16x16x32_bf16 v[72:75], a[200:203], v[208:211], 0\n\t"
    "v_exp_f32 v139, v139\n\t"
    "v_mfma_f32_16x16x32_bf16 v[104:107], a[200:203], v[212:215], 0\n\t"
    "ds_read_b128 a[156:159], v227 offset:61440\n\t"
    "v_cvt_pk_bf16_f32 v205, v138, v139\n\t"
    "v_mfma_f32_16x16x32_bf16 v[76:79], a[204:207], v[208:211], 0\n\t"
    "v_exp_f32 v140, v140\n\t"
    "v_mfma_f32_16x16x32_bf16 v[108:111], a[204:207], v[212:215], 0\n\t"
    "ds_read_b128 a[180:183], v225 offset:20480\n\t"
    "buffer_load_dwordx4 v236, s[64:67], s81 offen nt lds\n\t"
    "v_mfma_f32_16x16x32_bf16 v[80:83], a[208:211], v[208:211], 0\n\t"
    "v_exp_f32 v141, v141\n\t"
    "v_mfma_f32_16x16x32_bf16 v[112:115], a[208:211], v[212:215], 0\n\t"
    "ds_read_b128 a[184:187], v226 offset:20480\n\t"
    "v_cvt_pk_bf16_f32 v206, v140, v141\n\t"
    "v_mfma_f32_16x16x32_bf16 v[84:87], a[212:215], v[208:211], 0\n\t"
    "v_exp_f32 v142, v142\n\t"
    "v_mfma_f32_16x16x32_bf16 v[116:119], a[212:215], v[212:215], 0\n\t"
    "ds_read_b128 a[188:191], v227 offset:20480\n\t"
    "v_mfma_f32_16x16x32_bf16 v[88:91], a[216:219], v[208:211], 0\n\t"
    "v_exp_f32 v143, v143\n\t"
    "v_mfma_f32_16x16x32_bf16 v[120:123], a[216:219], v[212:215], 0\n\t"
    "ds_read_b64_tr_b16 a[192:193], v228 offset:8192\n\t"
    "v_cvt_pk_bf16_f32 v207, v142, v143\n\t"
    "v_mfma_f32_16x16x32_bf16 v[92:95], a[220:223], v[208:211], 0\n\t"
    "v_mfma_f32_16x16x32_bf16 v[124:127], a[220:223], v[212:215], 0\n\t"
    "ds_read_b64_tr_b16 a[194:195], v228 offset:12288\n\t"
    "buffer_load_dwordx4 v237, s[64:67], s81 offen offset:1024 nt lds\n\t"
    "v_mfma_f32_16x16x32_bf16 a[248:251], v[196:199], v[208:211], 0\n\t"
    "v_exp_f32 v144, v144\n\t"
    "v_mfma_f32_16x16x32_bf16 a[252:255], v[196:199], v[212:215], 0\n\t"
    "ds_read_b64_tr_b16 a[196:197], v229 offset:8192\n\t"
    "s_waitcnt lgkmcnt(2)\n\t"
    "v_mfma_f32_16x16x32_bf16 v[128:131], a[160:163], a[64:67], v[176:179]\n\t"
    "v_exp_f32 v145, v145\n\t"
    "v_mfma_f32_16x16x32_bf16 v[128:131], a[164:167], a[68:71], v[128:131]\n\t"
    "ds_read_b64_tr_b16 a[198:199], v229 offset:12288\n\t"
    "v_cvt_pk_bf16_f32 v208, v144, v145\n\t"
    "s_add_u32 m0, s80, 90112\n\t"
    "v_mfma_f32_16x16x32_bf16 v[128:131], a[168:171], a[72:75], v[128:131]\n\t"
    "v_exp_f32 v146, v146\n\t"
    "v_mfma_f32_16x16x32_bf16 v[128:131], a[172:175], a[76:79], v[128:131]\n\t"
    "ds_read_b64_tr_b16 a[200:201], v230 offset:8192\n\t"
    "s_mov_b32 s81, 24576\n\t"
    "v_mfma_f32_16x16x32_bf16 v[132:135], a[176:179], a[64:67], v[176:179]\n\t"
    "v_exp_f32 v147, v147\n\t"
    "v_mfma_f32_16x16x32_bf16 v[132:135], a[180:183], a[68:71], v[132:135]\n\t"
    "ds_read_b64_tr_b16 a[202:203], v230 offset:12288\n\t"
    "v_cvt_pk_bf16_f32 v209, v146, v147\n\t"
    "v_mfma_f32_16x16x32_bf16 v[132:135], a[184:187], a[72:75], v[132:135]\n\t"
    "v_exp_f32 v148, v148\n\t"
    "v_mfma_f32_16x16x32_bf16 v[132:135], a[188:191], a[76:79], v[132:135]\n\t"
    "ds_read_b64_tr_b16 a[204:205], v231 offset:8192\n\t"
    "buffer_load_dwordx4 v238, s[68:71], s81 offen nt lds\n\t"
    "v_mfma_f32_16x16x32_bf16 v[136:139], a[160:163], a[80:83], v[180:183]\n\t"
    "v_exp_f32 v149, v149\n\t"
    "v_mfma_f32_16x16x32_bf16 v[136:139], a[164:167], a[84:87], v[136:139]\n\t"
    "ds_read_b64_tr_b16 a[206:207], v231 offset:12288\n\t"
    "v_cvt_pk_bf16_f32 v210, v148, v149\n\t"
    "v_mfma_f32_16x16x32_bf16 v[136:139], a[168:171], a[88:91], v[136:139]\n\t"
    "v_exp_f32 v150, v150\n\t"
    "v_mfma_f32_16x16x32_bf16 v[136:139], a[172:175], a[92:95], v[136:139]\n\t"
    "ds_read_b64_tr_b16 a[208:209], v232 offset:8192\n\t"
    "v_mfma_f32_16x16x32_bf16 v[140:143], a[176:179], a[80:83], v[180:183]\n\t"
    "v_exp_f32 v151, v151\n\t"
    "v_mfma_f32_16x16x32_bf16 v[140:143], a[180:183], a[84:87], v[140:143]\n\t"
    "ds_read_b64_tr_b16 a[210:211], v232 offset:12288\n\t"
    "v_cvt_pk_bf16_f32 v211, v150, v151\n\t"
    "v_mfma_f32_16x16x32_bf16 v[140:143], a[184:187], a[88:91], v[140:143]\n\t"
    "v_exp_f32 v152, v152\n\t"
    "v_mfma_f32_16x16x32_bf16 v[140:143], a[188:191], a[92:95], v[140:143]\n\t"
    "ds_read_b64_tr_b16 a[212:213], v233 offset:8192\n\t"
    "buffer_load_dwordx4 v239, s[68:71], s81 offen offset:1024 nt lds\n\t"
    "s_waitcnt lgkmcnt(2)\n\t"
    "v_mfma_f32_16x16x32_bf16 v[0:3], a[192:195], v[200:203], v[0:3]\n\t"
    "v_exp_f32 v153, v153\n\t"
    "v_mfma_f32_16x16x32_bf16 v[32:35], a[192:195], v[204:207], v[32:35]\n\t"
    "ds_read_b64_tr_b16 a[214:215], v233 offset:12288\n\t"
    "v_cvt_pk_bf16_f32 v212, v152, v153\n\t"
    "v_mfma_f32_16x16x32_bf16 v[4:7], a[196:199], v[200:203], v[4:7]\n\t"
    "v_exp_f32 v154, v154\n\t"
    "v_mfma_f32_16x16x32_bf16 v[36:39], a[196:199], v[204:207], v[36:39]\n\t"
    "ds_read_b64_tr_b16 a[216:217], v234 offset:8192\n\t"
    "v_mfma_f32_16x16x32_bf16 v[8:11], a[200:203], v[200:203], v[8:11]\n\t"
    "v_exp_f32 v155, v155\n\t"
    "v_mfma_f32_16x16x32_bf16 v[40:43], a[200:203], v[204:207], v[40:43]\n\t"
    "ds_read_b64_tr_b16 a[218:219], v234 offset:12288\n\t"
    "v_cvt_pk_bf16_f32 v213, v154, v155\n\t"
    "s_add_u32 m0, s80, 32768\n\t"
    "v_mfma_f32_16x16x32_bf16 v[12:15], a[204:207], v[200:203], v[12:15]\n\t"
    "v_exp_f32 v156, v156\n\t"
    "v_mfma_f32_16x16x32_bf16 v[44:47], a[204:207], v[204:207], v[44:47]\n\t"
    "ds_read_b64_tr_b16 a[220:221], v235 offset:8192\n\t"
    "s_mov_b32 s81, 32768\n\t"
    "s_waitcnt lgkmcnt(2)\n\t"
    "v_mfma_f32_16x16x32_bf16 v[16:19], a[208:211], v[200:203], v[16:19]\n\t"
    "v_exp_f32 v157, v157\n\t"
    "v_mfma_f32_16x16x32_bf16 v[48:51], a[208:211], v[204:207], v[48:51]\n\t"
    "ds_read_b64_tr_b16 a[222:223], v235 offset:12288\n\t"
    "v_cvt_pk_bf16_f32 v214, v156, v157\n\t"
    "v_mfma_f32_16x16x32_bf16 v[20:23], a[212:215], v[200:203], v[20:23]\n\t"
    "v_exp_f32 v158, v158\n\t"
    "v_mfma_f32_16x16x32_bf16 v[52:55], a[212:215], v[204:207], v[52:55]\n\t"
    "buffer_load_dwordx4 v236, s[64:67], s81 offen nt lds\n\t"
    "s_waitcnt lgkmcnt(1)\n\t"
    "v_mfma_f32_16x16x32_bf16 v[24:27], a[216:219], v[200:203], v[24:27]\n\t"
    "v_exp_f32 v159, v159\n\t"
    "v_mfma_f32_16x16x32_bf16 v[56:59], a[216:219], v[204:207], v[56:59]\n\t"
    "v_cvt_pk_bf16_f32 v215, v158, v159\n\t"
    "s_waitcnt lgkmcnt(0)\n\t"
    "v_mfma_f32_16x16x32_bf16 v[28:31], a[220:223], v[200:203], v[28:31]\n\t"
    "v_mfma_f32_16x16x32_bf16 v[60:63], a[220:223], v[204:207], v[60:63]\n\t"
    "v_mfma_f32_16x16x32_bf16 a[240:243], v[196:199], v[200:203], a[240:243]\n\t"
    "v_exp_f32 v128, v128\n\t"
    "v_mfma_f32_16x16x32_bf16 a[244:247], v[196:199], v[204:207], a[244:247]\n\t"
    "s_waitcnt vmcnt(3)\n\t"
    "s_barrier\n\t"
    "v_mfma_f32_16x16x32_bf16 v[144:147], a[160:163], a[96:99], v[184:187]\n\t"
    "v_exp_f32 v129, v129\n\t"
    "v_mfma_f32_16x16x32_bf16 v[144:147], a[164:167], a[100:103], v[144:147]\n\t"
    "v_cvt_pk_bf16_f32 v200, v128, v129\n\t"
    "v_mfma_f32_16x16x32_bf16 v[144:147], a[168:171], a[104:107], v[144:147]\n\t"
    "v_exp_f32 v130, v130\n\t"
    "v_mfma_f32_16x16x32_bf16 v[144:147], a[172:175], a[108:111], v[144:147]\n\t"
    "buffer_load_dwordx4 v237, s[64:67], s81 offen offset:1024 nt lds\n\t"
    "v_mfma_f32_16x16x32_bf16 v[148:151], a[176:179], a[96:99], v[184:187]\n\t"
    "v_exp_f32 v131, v131\n\t"
    "v_mfma_f32_16x16x32_bf16 v[148:151], a[180:183], a[100:103], v[148:151]\n\t"
    "v_cvt_pk_bf16_f32 v201, v130, v131\n\t"
    "v_mfma_f32_16x16x32_bf16 v[148:151], a[184:187], a[104:107], v[148:151]\n\t"
    "v_exp_f32 v132, v132\n\t"
    "v_mfma_f32_16x16x32_bf16 v[148:151], a[188:191], a[108:111], v[148:151]\n\t"
    "s_add_u32 m0, s80, 98304\n\t"
    "s_mov_b32 s81, 32768\n\t"
    "v_mfma_f32_16x16x32_bf16 v[152:155], a[160:163], a[112:115], v[188:191]\n\t"
    "v_exp_f32 v133, v133\n\t"
    "v_mfma_f32_16x16x32_bf16 v[152:155], a[164:167], a[116:119], v[152:155]\n\t"
    "ds_read_b128 a[160:163], v224 offset:24576\n\t"
    "v_cvt_pk_bf16_f32 v202, v132, v133\n\t"
    "v_mfma_f32_16x16x32_bf16 v[152:155], a[168:171], a[120:123], v[152:155]\n\t"
    "v_exp_f32 v134, v134\n\t"
    "v_mfma_f32_16x16x32_bf16 v[152:155], a[172:175], a[124:127], v[152:155]\n\t"
    "ds_read_b128 a[164:167], v225 offset:24576\n\t"
    "buffer_load_dwordx4 v238, s[68:71], s81 offen nt lds\n\t"
    "v_mfma_f32_16x16x32_bf16 v[156:159], a[176:179], a[112:115], v[188:191]\n\t"
    "v_exp_f32 v135, v135\n\t"
    "v_mfma_f32_16x16x32_bf16 v[156:159], a[180:183], a[116:119], v[156:159]\n\t"
    "ds_read_b128 a[168:171], v226 offset:24576\n\t"
    "v_cvt_pk_bf16_f32 v203, v134, v135\n\t"
    "v_mfma_f32_16x16x32_bf16 v[156:159], a[184:187], a[120:123], v[156:159]\n\t"
    "v_exp_f32 v136, v136\n\t"
    "v_mfma_f32_16x16x32_bf16 v[156:159], a[188:191], a[124:127], v[156:159]\n\t"
    "ds_read_b128 a[172:175], v227 offset:24576\n\t"
    "v_mfma_f32_16x16x32_bf16 v[64:67], a[192:195], v[208:211], v[64:67]\n\t"
    "v_exp_f32 v137, v137\n\t"
    "v_mfma_f32_16x16x32_bf16 v[96:99], a[192:195], v[212:215], v[96:99]\n\t"
    "ds_read_b128 a[176:179], v224 offset:28672\n\t"
    "v_cvt_pk_bf16_f32 v204, v136, v137\n\t"
    "v_mfma_f32_16x16x32_bf16 v[68:71], a[196:199], v[208:211], v[68:71]\n\t"
    "v_exp_f32 v138, v138\n\t"
    "v_mfma_f32_16x16x32_bf16 v[100:103], a[196:199], v[212:215], v[100:103]\n\t"
    "ds_read_b128 a[180:183], v225 offset:28672\n\t"
    "buffer_load_dwordx4 v239, s[68:71], s81 offen offset:1024 nt lds\n\t"
    "v_mfma_f32_16x16x32_bf16 v[72:75], a[200:203], v[208:211], v[72:75]\n\t"
    "v_exp_f32 v139, v139\n\t"
    "v_mfma_f32_16x16x32_bf16 v[104:107], a[200:203], v[212:215], v[104:107]\n\t"
    "ds_read_b128 a[184:187], v226 offset:28672\n\t"
    "v_cvt_pk_bf16_f32 v205, v138, v139\n\t"
    "v_mfma_f32_16x16x32_bf16 v[76:79], a[204:207], v[208:211], v[76:79]\n\t"
    "v_exp_f32 v140, v140\n\t"
    "v_mfma_f32_16x16x32_bf16 v[108:111], a[204:207], v[212:215], v[108:111]\n\t"
    "ds_read_b128 a[188:191], v227 offset:28672\n\t"
    "v_mfma_f32_16x16x32_bf16 v[80:83], a[208:211], v[208:211], v[80:83]\n\t"
    "v_exp_f32 v141, v141\n\t"
    "v_mfma_f32_16x16x32_bf16 v[112:115], a[208:211], v[212:215], v[112:115]\n\t"
    "ds_read_b64_tr_b16 a[192:193], v228 offset:16384\n\t"
    "v_cvt_pk_bf16_f32 v206, v140, v141\n\t"
    "s_add_u32 m0, s80, 40960\n\t"
    "v_mfma_f32_16x16x32_bf16 v[84:87], a[212:215], v[208:211], v[84:87]\n\t"
    "v_exp_f32 v142, v142\n\t"
    "v_mfma_f32_16x16x32_bf16 v[116:119], a[212:215], v[212:215], v[116:119]\n\t"
    "ds_read_b64_tr_b16 a[194:195], v228 offset:20480\n\t"
    "s_mov_b32 s81, 40960\n\t"
    "v_mfma_f32_16x16x32_bf16 v[88:91], a[216:219], v[208:211], v[88:91]\n\t"
    "v_exp_f32 v143, v143\n\t"
    "v_mfma_f32_16x16x32_bf16 v[120:123], a[216:219], v[212:215], v[120:123]\n\t"
    "ds_read_b64_tr_b16 a[196:197], v229 offset:16384\n\t"
    "v_cvt_pk_bf16_f32 v207, v142, v143\n\t"
    "v_mfma_f32_16x16x32_bf16 v[92:95], a[220:223], v[208:211], v[92:95]\n\t"
    "v_mfma_f32_16x16x32_bf16 v[124:127], a[220:223], v[212:215], v[124:127]\n\t"
    "ds_read_b64_tr_b16 a[198:199], v229 offset:20480\n\t"
    "buffer_load_dwordx4 v236, s[64:67], s81 offen nt lds\n\t"
    "v_mfma_f32_16x16x32_bf16 a[248:251], v[196:199], v[208:211], a[248:251]\n\t"
    "v_exp_f32 v144, v144\n\t"
    "v_mfma_f32_16x16x32_bf16 a[252:255], v[196:199], v[212:215], a[252:255]\n\t"
    "ds_read_b64_tr_b16 a[200:201], v230 offset:16384\n\t"
    "s_waitcnt lgkmcnt(2)\n\t"
    "v_mfma_f32_16x16x32_bf16 v[128:131], a[160:163], a[64:67], v[176:179]\n\t"
    "v_exp_f32 v145, v145\n\t"
    "v_mfma_f32_16x16x32_bf16 v[128:131], a[164:167], a[68:71], v[128:131]\n\t"
    "ds_read_b64_tr_b16 a[202:203], v230 offset:20480\n\t"
    "v_cvt_pk_bf16_f32 v208, v144, v145\n\t"
    "v_mfma_f32_16x16x32_bf16 v[128:131], a[168:171], a[72:75], v[128:131]\n\t"
    "v_exp_f32 v146, v146\n\t"
    "v_mfma_f32_16x16x32_bf16 v[128:131], a[172:175], a[76:79], v[128:131]\n\t"
    "ds_read_b64_tr_b16 a[204:205], v231 offset:16384\n\t"
    "v_mfma_f32_16x16x32_bf16 v[132:135], a[176:179], a[64:67], v[176:179]\n\t"
    "v_exp_f32 v147, v147\n\t"
    "v_mfma_f32_16x16x32_bf16 v[132:135], a[180:183], a[68:71], v[132:135]\n\t"
    "ds_read_b64_tr_b16 a[206:207], v231 offset:20480\n\t"
    "v_cvt_pk_bf16_f32 v209, v146, v147\n\t"
    "v_mfma_f32_16x16x32_bf16 v[132:135], a[184:187], a[72:75], v[132:135]\n\t"
    "v_exp_f32 v148, v148\n\t"
    "v_mfma_f32_16x16x32_bf16 v[132:135], a[188:191], a[76:79], v[132:135]\n\t"
    "ds_read_b64_tr_b16 a[208:209], v232 offset:16384\n\t"
    "buffer_load_dwordx4 v237, s[64:67], s81 offen offset:1024 nt lds\n\t"
    "v_mfma_f32_16x16x32_bf16 v[136:139], a[160:163], a[80:83], v[180:183]\n\t"
    "v_exp_f32 v149, v149\n\t"
    "v_mfma_f32_16x16x32_bf16 v[136:139], a[164:167], a[84:87], v[136:139]\n\t"
    "ds_read_b64_tr_b16 a[210:211], v232 offset:20480\n\t"
    "v_cvt_pk_bf16_f32 v210, v148, v149\n\t"
    "v_mfma_f32_16x16x32_bf16 v[136:139], a[168:171], a[88:91], v[136:139]\n\t"
    "v_exp_f32 v150, v150\n\t"
    "v_mfma_f32_16x16x32_bf16 v[136:139], a[172:175], a[92:95], v[136:139]\n\t"
    "ds_read_b64_tr_b16 a[212:213], v233 offset:16384\n\t"
    "v_mfma_f32_16x16x32_bf16 v[140:143], a[176:179], a[80:83], v[180:183]\n\t"
    "v_exp_f32 v151, v151\n\t"
    "v_mfma_f32_16x16x32_bf16 v[140:143], a[180:183], a[84:87], v[140:143]\n\t"
    "ds_read_b64_tr_b16 a[214:215], v233 offset:20480\n\t"
    "v_cvt_pk_bf16_f32 v211, v150, v151\n\t"
    "s_add_u32 m0, s80, 106496\n\t"
    "v_mfma_f32_16x16x32_bf16 v[140:143], a[184:187], a[88:91], v[140:143]\n\t"
    "v_exp_f32 v152, v152\n\t"
    "v_mfma_f32_16x16x32_bf16 v[140:143], a[188:191], a[92:95], v[140:143]\n\t"
    "ds_read_b64_tr_b16 a[216:217], v234 offset:16384\n\t"
    "s_mov_b32 s81, 40960\n\t"
    "v_mfma_f32_16x16x32_bf16 v[0:3], a[192:195], v[200:203], v[0:3]\n\t"
    "v_exp_f32 v153, v153\n\t"
    "v_mfma_f32_16x16x32_bf16 v[32:35], a[192:195], v[204:207], v[32:35]\n\t"
    "ds_read_b64_tr_b16 a[218:219], v234 offset:20480\n\t"
    "v_cvt_pk_bf16_f32 v212, v152, v153\n\t"
    "s_waitcnt lgkmcnt(2)\n\t"
    "v_mfma_f32_16x16x32_bf16 v[4:7], a[196:199], v[200:203], v[4:7]\n\t"
    "v_exp_f32 v154, v154\n\t"
    "v_mfma_f32_16x16x32_bf16 v[36:39], a[196:199], v[204:207], v[36:39]\n\t"
    "ds_read_b64_tr_b16 a[220:221], v235 offset:16384\n\t"
    "buffer_load_dwordx4 v238, s[68:71], s81 offen nt lds\n\t"
    "v_mfma_f32_16x16x32_bf16 v[8:11], a[200:203], v[200:203], v[8:11]\n\t"
    "v_exp_f32 v155, v155\n\t"
    "v_mfma_f32_16x16x32_bf16 v[40:43], a[200:203], v[204:207], v[40:43]\n\t"
    "ds_read_b64_tr_b16 a[222:223], v235 offset:20480\n\t"
    "v_cvt_pk_bf16_f32 v213, v154, v155\n\t"
    "v_mfma_f32_16x16x32_bf16 v[12:15], a[204:207], v[200:203], v[12:15]\n\t"
    "v_exp_f32 v156, v156\n\t"
    "v_mfma_f32_16x16x32_bf16 v[44:47], a[204:207], v[204:207], v[44:47]\n\t"
    "v_add_u32 v240, s63, v224\n\t"
    "ds_read_b128 a[224:227], v240 offset:0\n\t"
    "v_mfma_f32_16x16x32_bf16 v[16:19], a[208:211], v[200:203], v[16:19]\n\t"
    "v_exp_f32 v157, v157\n\t"
    "v_mfma_f32_16x16x32_bf16 v[48:51], a[208:211], v[204:207], v[48:51]\n\t"
    "v_add_u32 v241, s63, v225\n\t"
    "ds_read_b128 a[228:231], v241 offset:0\n\t"
    "v_cvt_pk_bf16_f32 v214, v156, v157\n\t"
    "v_mfma_f32_16x16x32_bf16 v[20:23], a[212:215], v[200:203], v[20:23]\n\t"
    "v_exp_f32 v158, v158\n\t"
    "v_mfma_f32_16x16x32_bf16 v[52:55], a[212:215], v[204:207], v[52:55]\n\t"
    "v_add_u32 v242, s63, v226\n\t"
    "ds_read_b128 a[232:235], v242 offset:0\n\t"
    "s_waitcnt lgkmcnt(2)\n\t"
    "v_mfma_f32_16x16x32_bf16 v[24:27], a[216:219], v[200:203], v[24:27]\n\t"
    "v_exp_f32 v159, v159\n\t"
    "v_mfma_f32_16x16x32_bf16 v[56:59], a[216:219], v[204:207], v[56:59]\n\t"
    "v_add_u32 v243, s63, v227\n\t"
    "ds_read_b128 a[236:239], v243 offset:0\n\t"
    "v_cvt_pk_bf16_f32 v215, v158, v159\n\t"
    "v_mfma_f32_16x16x32_bf16 v[28:31], a[220:223], v[200:203], v[28:31]\n\t"
    "v_mfma_f32_16x16x32_bf16 v[60:63], a[220:223], v[204:207], v[60:63]\n\t"
    "buffer_load_dwordx4 v239, s[68:71], s81 offen offset:1024 nt lds\n\t"
    "v_mfma_f32_16x16x32_bf16 a[240:243], v[196:199], v[200:203], a[240:243]\n\t"
    "v_exp_f32 v128, v128\n\t"
    "v_mfma_f32_16x16x32_bf16 a[244:247], v[196:199], v[204:207], a[244:247]\n\t"
    "v_mfma_f32_16x16x32_bf16 v[160:163], a[224:227], a[128:131], 0\n\t"
    "v_exp_f32 v129, v129\n\t"
    "s_waitcnt lgkmcnt(0)\n\t"
    "v_mfma_f32_16x16x32_bf16 v[160:163], a[228:231], a[132:135], v[160:163]\n\t"
    "v_cvt_pk_bf16_f32 v200, v128, v129\n\t"
    "v_mfma_f32_16x16x32_bf16 v[160:163], a[232:235], a[136:139], v[160:163]\n\t"
    "v_exp_f32 v130, v130\n\t"
    "v_mfma_f32_16x16x32_bf16 v[160:163], a[236:239], a[140:143], v[160:163]\n\t"
    "s_add_u32 m0, s80, 49152\n\t"
    "s_mov_b32 s81, 49152\n\t"
    "v_mfma_f32_16x16x32_bf16 v[168:171], a[224:227], a[144:147], 0\n\t"
    "v_exp_f32 v131, v131\n\t"
    "v_mfma_f32_16x16x32_bf16 v[168:171], a[228:231], a[148:151], v[168:171]\n\t"
    "ds_read_b128 a[224:227], v240 offset:4096\n\t"
    "v_cvt_pk_bf16_f32 v201, v130, v131\n\t"
    "v_mfma_f32_16x16x32_bf16 v[168:171], a[232:235], a[152:155], v[168:171]\n\t"
    "v_exp_f32 v132, v132\n\t"
    "v_mfma_f32_16x16x32_bf16 v[168:171], a[236:239], a[156:159], v[168:171]\n\t"
    "ds_read_b128 a[228:231], v241 offset:4096\n\t"
    "buffer_load_dwordx4 v236, s[64:67], s81 offen nt lds\n\t"
    "s_waitcnt vmcnt(7)\n\t"
    "s_barrier\n\t"
    "v_mfma_f32_16x16x32_bf16 v[144:147], a[160:163], a[96:99], v[184:187]\n\t"
    "v_exp_f32 v133, v133\n\t"
    "v_mfma_f32_16x16x32_bf16 v[144:147], a[164:167], a[100:103], v[144:147]\n\t"
    "ds_read_b128 a[232:235], v242 offset:4096\n\t"
    "v_cvt_pk_bf16_f32 v202, v132, v133\n\t"
    "v_mfma_f32_16x16x32_bf16 v[144:147], a[168:171], a[104:107], v[144:147]\n\t"
    "v_exp_f32 v134, v134\n\t"
    "v_mfma_f32_16x16x32_bf16 v[144:147], a[172:175], a[108:111], v[144:147]\n\t"
    "ds_read_b128 a[236:239], v243 offset:4096\n\t"
    "buffer_load_dwordx4 v237, s[64:67], s81 offen offset:1024 nt lds\n\t"
    "v_mfma_f32_16x16x32_bf16 v[148:151], a[176:179], a[96:99], v[184:187]\n\t"
    "v_exp_f32 v135, v135\n\t"
    "v_mfma_f32_16x16x32_bf16 v[148:151], a[180:183], a[100:103], v[148:151]\n\t"
    "v_cvt_pk_bf16_f32 v203, v134, v135\n\t"
    "v_mfma_f32_16x16x32_bf16 v[148:151], a[184:187], a[104:107], v[148:151]\n\t"
    "v_exp_f32 v136, v136\n\t"
    "v_mfma_f32_16x16x32_bf16 v[148:151], a[188:191], a[108:111], v[148:151]\n\t"
    "v_mfma_f32_16x16x32_bf16 v[152:155], a[160:163], a[112:115], v[188:191]\n\t"
    "v_exp_f32 v137, v137\n\t"
    "v_mfma_f32_16x16x32_bf16 v[152:155], a[164:167], a[116:119], v[152:155]\n\t"
    "ds_read_b128 a[160:163], v224 offset:32768\n\t"
    "v_cvt_pk_bf16_f32 v204, v136, v137\n\t"
    "s_add_u32 m0, s80, 114688\n\t"
    "v_mfma_f32_16x16x32_bf16 v[152:155], a[168:171], a[120:123], v[152:155]\n\t"
    "v_exp_f32 v138, v138\n\t"
    "v_mfma_f32_16x16x32_bf16 v[152:155], a[172:175], a[124:127], v[152:155]\n\t"
    "ds_read_b128 a[164:167], v225 offset:32768\n\t"
    "s_mov_b32 s81, 49152\n\t"
    "v_mfma_f32_16x16x32_bf16 v[156:159], a[176:179], a[112:115], v[188:191]\n\t"
    "v_exp_f32 v139, v139\n\t"
    "v_mfma_f32_16x16x32_bf16 v[156:159], a[180:183], a[116:119], v[156:159]\n\t"
    "ds_read_b128 a[168:171], v226 offset:32768\n\t"
    "v_cvt_pk_bf16_f32 v205, v138, v139\n\t"
    "v_mfma_f32_16x16x32_bf16 v[156:159], a[184:187], a[120:123], v[156:159]\n\t"
    "v_exp_f32 v140, v140\n\t"
    "v_mfma_f32_16x16x32_bf16 v[156:159], a[188:191], a[124:127], v[156:159]\n\t"
    "ds_read_b128 a[172:175], v227 offset:32768\n\t"
    "buffer_load_dwordx4 v238, s[68:71], s81 offen nt lds\n\t"
    "s_waitcnt lgkmcnt(2)\n\t"
    "v_mfma_f32_16x16x32_bf16 v[164:167], a[224:227], a[128:131], 0\n\t"
    "v_exp_f32 v141, v141\n\t"
    "v_mfma_f32_16x16x32_bf16 v[164:167], a[228:231], a[132:135], v[164:167]\n\t"
    "ds_read_b128 a[176:179], v224 offset:36864\n\t"
    "v_cvt_pk_bf16_f32 v206, v140, v141\n\t"
    "v_mfma_f32_16x16x32_bf16 v[164:167], a[232:235], a[136:139], v[164:167]\n\t"
    "v_exp_f32 v142, v142\n\t"
    "v_mfma_f32_16x16x32_bf16 v[164:167], a[236:239], a[140:143], v[164:167]\n\t"
    "ds_read_b128 a[180:183], v225 offset:36864\n\t"
    "v_mfma_f32_16x16x32_bf16 v[172:175], a[224:227], a[144:147], 0\n\t"
    "v_exp_f32 v143, v143\n\t"
    "v_mfma_f32_16x16x32_bf16 v[172:175], a[228:231], a[148:151], v[172:175]\n\t"
    "ds_read_b128 a[184:187], v226 offset:36864\n\t"
    "v_cvt_pk_bf16_f32 v207, v142, v143\n\t"
    "v_mfma_f32_16x16x32_bf16 v[172:175], a[232:235], a[152:155], v[172:175]\n\t"
    "v_mfma_f32_16x16x32_bf16 v[172:175], a[236:239], a[156:159], v[172:175]\n\t"
    "ds_read_b128 a[188:191], v227 offset:36864\n\t"
    "buffer_load_dwordx4 v239, s[68:71], s81 offen offset:1024 nt lds\n\t"
    "v_mfma_f32_16x16x32_bf16 v[64:67], a[192:195], v[208:211], v[64:67]\n\t"
    "v_mfma_f32_16x16x32_bf16 v[96:99], a[192:195], v[212:215], v[96:99]\n\t"
    "ds_read_b64_tr_b16 a[192:193], v228 offset:24576\n\t"
    "v_mfma_f32_16x16x32_bf16 v[68:71], a[196:199], v[208:211], v[68:71]\n\t"
    "v_mfma_f32_16x16x32_bf16 v[100:103], a[196:199], v[212:215], v[100:103]\n\t"
    "ds_read_b64_tr_b16 a[194:195], v228 offset:28672\n\t"
    "v_mfma_f32_16x16x32_bf16 v[72:75], a[200:203], v[208:211], v[72:75]\n\t"
    "v_mfma_f32_16x16x32_bf16 v[104:107], a[200:203], v[212:215], v[104:107]\n\t"
    "ds_read_b64_tr_b16 a[196:197], v229 offset:24576\n\t"
    "s_add_u32 m0, s80, 57344\n\t"
    "s_mov_b32 s81, 57344\n\t"
    "v_mfma_f32_16x16x32_bf16 v[76:79], a[204:207], v[208:211], v[76:79]\n\t"
    "v_mfma_f32_16x16x32_bf16 v[108:111], a[204:207], v[212:215], v[108:111]\n\t"
    "ds_read_b64_tr_b16 a[198:199], v229 offset:28672\n\t"
    "buffer_load_dwordx4 v236, s[64:67], s81 offen nt lds\n\t"
    "v_mfma_f32_16x16x32_bf16 v[80:83], a[208:211], v[208:211], v[80:83]\n\t"
    "v_mfma_f32_16x16x32_bf16 v[112:115], a[208:211], v[212:215], v[112:115]\n\t"
    "ds_read_b64_tr_b16 a[200:201], v230 offset:24576\n\t"
    "v_mfma_f32_16x16x32_bf16 v[84:87], a[212:215], v[208:211], v[84:87]\n\t"
    "v_mfma_f32_16x16x32_bf16 v[116:119], a[212:215], v[212:215], v[116:119]\n\t"
    "ds_read_b64_tr_b16 a[202:203], v230 offset:28672\n\t"
    "v_mfma_f32_16x16x32_bf16 v[88:91], a[216:219], v[208:211], v[88:91]\n\t"
    "v_mfma_f32_16x16x32_bf16 v[120:123], a[216:219], v[212:215], v[120:123]\n\t"
    "ds_read_b64_tr_b16 a[204:205], v231 offset:24576\n\t"
    "v_mfma_f32_16x16x32_bf16 v[92:95], a[220:223], v[208:211], v[92:95]\n\t"
    "v_mfma_f32_16x16x32_bf16 v[124:127], a[220:223], v[212:215], v[124:127]\n\t"
    "ds_read_b64_tr_b16 a[206:207], v231 offset:28672\n\t"
    "buffer_load_dwordx4 v237, s[64:67], s81 offen offset:1024 nt lds\n\t"
    "v_mfma_f32_16x16x32_bf16 a[248:251], v[196:199], v[208:211], a[248:251]\n\t"
    "v_exp_f32 v144, v144\n\t"
    "v_mfma_f32_16x16x32_bf16 a[252:255], v[196:199], v[212:215], a[252:255]\n\t"
    "ds_read_b64_tr_b16 a[208:209], v232 offset:24576\n\t"
    "v_exp_f32 v145, v145\n\t"
    "v_mfma_f32_16x16x32_bf16 v[128:131], a[160:163], a[64:67], v[176:179]\n\t"
    "v_exp_f32 v146, v146\n\t"
    "v_cvt_pk_bf16_f32 v208, v144, v145\n\t"
    "v_mfma_f32_16x16x32_bf16 v[128:131], a[164:167], a[68:71], v[128:131]\n\t"
    "ds_read_b64_tr_b16 a[210:211], v232 offset:28672\n\t"
    "v_exp_f32 v147, v147\n\t"
    "s_waitcnt lgkmcnt(2)\n\t"
    "v_mfma_f32_16x16x32_bf16 v[128:131], a[168:171], a[72:75], v[128:131]\n\t"
    "v_exp_f32 v148, v148\n\t"
    "v_cvt_pk_bf16_f32 v209, v146, v147\n\t"
    "v_mfma_f32_16x16x32_bf16 v[128:131], a[172:175], a[76:79], v[128:131]\n\t"
    "ds_read_b64_tr_b16 a[212:213], v233 offset:24576\n\t"
    "v_exp_f32 v149, v149\n\t"
    "v_mfma_f32_16x16x32_bf16 v[132:135], a[176:179], a[64:67], v[176:179]\n\t"
    "v_exp_f32 v150, v150\n\t"
    "v_cvt_pk_bf16_f32 v210, v148, v149\n\t"
    "v_mfma_f32_16x16x32_bf16 v[132:135], a[180:183], a[68:71], v[132:135]\n\t"
    "ds_read_b64_tr_b16 a[214:215], v233 offset:28672\n\t"
    "v_exp_f32 v151, v151\n\t"
    "v_mfma_f32_16x16x32_bf16 v[132:135], a[184:187], a[72:75], v[132:135]\n\t"
    "v_exp_f32 v152, v152\n\t"
    "v_cvt_pk_bf16_f32 v211, v150, v151\n\t"
    "v_mfma_f32_16x16x32_bf16 v[132:135], a[188:191], a[76:79], v[132:135]\n\t"
    "ds_read_b64_tr_b16 a[216:217], v234 offset:24576\n\t"
    "v_exp_f32 v153, v153\n\t"
    "v_mfma_f32_16x16x32_bf16 v[136:139], a[160:163], a[80:83], v[180:183]\n\t"
    "v_exp_f32 v154, v154\n\t"
    "v_cvt_pk_bf16_f32 v212, v152, v153\n\t"
    "v_mfma_f32_16x16x32_bf16 v[136:139], a[164:167], a[84:87], v[136:139]\n\t"
    "ds_read_b64_tr_b16 a[218:219], v234 offset:28672\n\t"
    "v_exp_f32 v155, v155\n\t"
    "v_mfma_f32_16x16x32_bf16 v[136:139], a[168:171], a[88:91], v[136:139]\n\t"
    "v_exp_f32 v156, v156\n\t"
    "v_cvt_pk_bf16_f32 v213, v154, v155\n\t"
    "v_mfma_f32_16x16x32_bf16 v[136:139], a[172:175], a[92:95], v[136:139]\n\t"
    "ds_read_b64_tr_b16 a[220:221], v235 offset:24576\n\t"
    "v_exp_f32 v157, v157\n\t"
    "v_mfma_f32_16x16x32_bf16 v[140:143], a[176:179], a[80:83], v[180:183]\n\t"
    "v_exp_f32 v158, v158\n\t"
    "v_cvt_pk_bf16_f32 v214, v156, v157\n\t"
    "v_mfma_f32_16x16x32_bf16 v[140:143], a[180:183], a[84:87], v[140:143]\n\t"
    "ds_read_b64_tr_b16 a[222:223], v235 offset:28672\n\t"
    "v_exp_f32 v159, v159\n\t"
    "v_mfma_f32_16x16x32_bf16 v[140:143], a[184:187], a[88:91], v[140:143]\n\t"
    "v_max3_f32 v248, v160, v161, v162\n\t"
    "v_max3_f32 v249, v163, v164, v165\n\t"
    "v_cvt_pk_bf16_f32 v215, v158, v159\n\t"
    "v_mfma_f32_16x16x32_bf16 v[140:143], a[188:191], a[92:95], v[140:143]\n\t"
    "v_add_u32 v244, s63, v228\n\t"
    "ds_read_b64_tr_b16 a[224:225], v244 offset:0\n\t"
    "v_max3_f32 v248, v248, v166, v167\n\t"
    "v_max_f32 v248, v248, v249\n\t"
    "v_mfma_f32_16x16x32_bf16 v[0:3], a[192:195], v[200:203], v[0:3]\n\t"
    "v_mov_b32 v249, v248\n\t"
    "s_nop 1\n\t"
    "v_mfma_f32_16x16x32_bf16 v[32:35], a[192:195], v[204:207], v[32:35]\n\t"
    "ds_read_b64_tr_b16 a[226:227], v244 offset:4096\n\t"
    "v_permlane32_swap_b32 v248, v249\n\t"
    "v_max_f32 v248, v248, v249\n\t"
    "v_mfma_f32_16x16x32_bf16 v[4:7], a[196:199], v[200:203], v[4:7]\n\t"
    "v_mov_b32 v249, v248\n\t"
    "s_nop 1\n\t"
    "v_mfma_f32_16x16x32_bf16 v[36:39], a[196:199], v[204:207], v[36:39]\n\t"
    "v_add_u32 v245, s63, v229\n\t"
    "ds_read_b64_tr_b16 a[228:229], v245 offset:0\n\t"
    "v_permlane16_swap_b32 v248, v249\n\t"
    "v_min_f32 v248, -v248, -v249\n\t"
    "v_mfma_f32_16x16x32_bf16 v[8:11], a[200:203], v[200:203], v[8:11]\n\t"
    "v_mul_f32 v192, s84, v248\n\t"
    "v_max3_f32 v250, v168, v169, v170\n\t"
    "v_mfma_f32_16x16x32_bf16 v[40:43], a[200:203], v[204:207], v[40:43]\n\t"
    "ds_read_b64_tr_b16 a[230:231], v245 offset:4096\n\t"
    "v_max3_f32 v251, v171, v172, v173\n\t"
    "v_max3_f32 v250, v250, v174, v175\n\t"
    "v_mfma_f32_16x16x32_bf16 v[12:15], a[204:207], v[200:203], v[12:15]\n\t"
    "v_max_f32 v250, v250, v251\n\t"
    "v_mov_b32 v251, v250\n\t"
    "v_mfma_f32_16x16x32_bf16 v[44:47], a[204:207], v[204:207], v[44:47]\n\t"
    "v_add_u32 v246, s63, v230\n\t"
    "ds_read_b64_tr_b16 a[232:233], v246 offset:0\n\t"
    "s_nop 1\n\t"
    "v_permlane32_swap_b32 v250, v251\n\t"
    "s_waitcnt lgkmcnt(2)\n\t"
    "v_mfma_f32_16x16x32_bf16 v[16:19], a[208:211], v[200:203], v[16:19]\n\t"
    "v_max_f32 v250, v250, v251\n\t"
    "v_mov_b32 v251, v250\n\t"
    "v_mfma_f32_16x16x32_bf16 v[48:51], a[208:211], v[204:207], v[48:51]\n\t"
    "ds_read_b64_tr_b16 a[234:235], v246 offset:4096\n\t"
    "s_nop 1\n\t"
    "v_permlane16_swap_b32 v250, v251\n\t"
    "v_mfma_f32_16x16x32_bf16 v[20:23], a[212:215], v[200:203], v[20:23]\n\t"
    "v_min_f32 v250, -v250, -v251\n\t"
    "v_mul_f32 v193, s84, v250\n\t"
    "v_mfma_f32_16x16x32_bf16 v[52:55], a[212:215], v[204:207], v[52:55]\n\t"
    "v_add_u32 v247, s63, v231\n\t"
    "ds_read_b64_tr_b16 a[236:237], v247 offset:0\n\t"
    "v_fma_f32 v160, v160, s84, v192\n\t"
    "v_exp_f32 v160, v160\n\t"
    "v_fma_f32 v161, v161, s84, v192\n\t"
    "v_exp_f32 v161, v161\n\t"
    "v_mfma_f32_16x16x32_bf16 v[24:27], a[216:219], v[200:203], v[24:27]\n\t"
    "v_fma_f32 v162, v162, s84, v192\n\t"
    "v_exp_f32 v162, v162\n\t"
    "v_fma_f32 v163, v163, s84, v192\n\t"
    "v_exp_f32 v163, v163\n\t"
    "v_cvt_pk_bf16_f32 v160, v160, v161\n\t"
    "v_mfma_f32_16x16x32_bf16 v[56:59], a[216:219], v[204:207], v[56:59]\n\t"
    "ds_read_b64_tr_b16 a[238:239], v247 offset:4096\n\t"
    "v_fma_f32 v164, v164, s84, v192\n\t"
    "v_exp_f32 v164, v164\n\t"
    "v_fma_f32 v165, v165, s84, v192\n\t"
    "v_exp_f32 v165, v165\n\t"
    "v_cvt_pk_bf16_f32 v161, v162, v163\n\t"
    "v_mfma_f32_16x16x32_bf16 v[28:31], a[220:223], v[200:203], v[28:31]\n\t"
    "v_fma_f32 v166, v166, s84, v192\n\t"
    "v_exp_f32 v166, v166\n\t"
    "v_fma_f32 v167, v167, s84, v192\n\t"
    "v_exp_f32 v167, v167\n\t"
    "v_cvt_pk_bf16_f32 v162, v164, v165\n\t"
    "v_mfma_f32_16x16x32_bf16 v[60:63], a[220:223], v[204:207], v[60:63]\n\t"
    "v_fma_f32 v168, v168, s84, v193\n\t"
    "v_exp_f32 v168, v168\n\t"
    "v_fma_f32 v169, v169, s84, v193\n\t"
    "v_exp_f32 v169, v169\n\t"
    "v_cvt_pk_bf16_f32 v163, v166, v167\n\t"
    "v_mfma_f32_16x16x32_bf16 a[240:243], v[196:199], v[200:203], a[240:243]\n\t"
    "v_fma_f32 v170, v170, s84, v193\n\t"
    "v_exp_f32 v170, v170\n\t"
    "v_fma_f32 v171, v171, s84, v193\n\t"
    "v_exp_f32 v171, v171\n\t"
    "v_cvt_pk_bf16_f32 v168, v168, v169\n\t"
    "v_mfma_f32_16x16x32_bf16 a[244:247], v[196:199], v[204:207], a[244:247]\n\t"
    "v_fma_f32 v172, v172, s84, v193\n\t"
    "v_exp_f32 v172, v172\n\t"
    "v_fma_f32 v173, v173, s84, v193\n\t"
    "v_exp_f32 v173, v173\n\t"
    "v_cvt_pk_bf16_f32 v169, v170, v171\n\t"
    "v_fma_f32 v174, v174, s84, v193\n\t"
    "v_exp_f32 v174, v174\n\t"
    "v_fma_f32 v175, v175, s84, v193\n\t"
    "v_exp_f32 v175, v175\n\t"
    "s_nop 0\n\t"
    "v_cvt_pk_bf16_f32 v170, v172, v173\n\t"
    "v_cvt_pk_bf16_f32 v171, v174, v175\n\t"
    "s_nop 1\n\t"
    "v_mfma_f32_16x16x32_bf16 a[0:3], a[224:227], v[160:163], 0\n\t"
    "v_exp_f32 v128, v128\n\t"
    "v_mfma_f32_16x16x32_bf16 a[32:35], a[224:227], v[168:171], 0\n\t"
    "v_add_u32 v244, s63, v232\n\t"
    "ds_read_b64_tr_b16 a[224:225], v244 offset:0\n\t"
    "s_waitcnt lgkmcnt(1)\n\t"
    "v_mfma_f32_16x16x32_bf16 a[4:7], a[228:231], v[160:163], 0\n\t"
    "v_exp_f32 v129, v129\n\t"
    "v_mfma_f32_16x16x32_bf16 a[36:39], a[228:231], v[168:171], 0\n\t"
    "ds_read_b64_tr_b16 a[226:227], v244 offset:4096\n\t"
    "v_cvt_pk_bf16_f32 v200, v128, v129\n\t"
    "v_mfma_f32_16x16x32_bf16 a[8:11], a[232:235], v[160:163], 0\n\t"
    "v_exp_f32 v130, v130\n\t"
    "v_mfma_f32_16x16x32_bf16 a[40:43], a[232:235], v[168:171], 0\n\t"
    "v_add_u32 v245, s63, v233\n\t"
    "ds_read_b64_tr_b16 a[228:229], v245 offset:0\n\t"
    "v_mfma_f32_16x16x32_bf16 a[12:15], a[236:239], v[160:163], 0\n\t"
    "v_exp_f32 v131, v131\n\t"
    "v_mfma_f32_16x16x32_bf16 a[44:47], a[236:239], v[168:171], 0\n\t"
    "ds_read_b64_tr_b16 a[230:231], v245 offset:4096\n\t"
    "v_cvt_pk_bf16_f32 v201, v130, v131\n\t"
    "v_mfma_f32_16x16x32_bf16 v[216:219], v[196:199], v[160:163], 0\n\t"
    "v_exp_f32 v132, v132\n\t"
    "v_mfma_f32_16x16x32_bf16 v[220:223], v[196:199], v[168:171], 0\n\t"
    "v_add_u32 v246, s63, v234\n\t"
    "ds_read_b64_tr_b16 a[232:233], v246 offset:0\n\t"
    "s_waitcnt vmcnt(8)\n\t"
    "s_barrier\n\t"
    "v_mfma_f32_16x16x32_bf16 v[144:147], a[160:163], a[96:99], v[184:187]\n\t"
    "v_exp_f32 v133, v133\n\t"
    "v_mfma_f32_16x16x32_bf16 v[144:147], a[164:167], a[100:103], v[144:147]\n\t"
    "ds_read_b64_tr_b16 a[234:235], v246 offset:4096\n\t"
    "v_cvt_pk_bf16_f32 v202, v132, v133\n\t"
    "s_add_u32 m0, s80, 122880\n\t"
    "v_mfma_f32_16x16x32_bf16 v[144:147], a[168:171], a[104:107], v[144:147]\n\t"
    "v_exp_f32 v134, v134\n\t"
    "v_mfma_f32_16x16x32_bf16 v[144:147], a[172:175], a[108:111], v[144:147]\n\t"
    "v_add_u32 v247, s63, v235\n\t"
    "ds_read_b64_tr_b16 a[236:237], v247 offset:0\n\t"
    "s_mov_b32 s81, 57344\n\t"
    "v_mfma_f32_16x16x32_bf16 v[148:151], a[176:179], a[96:99], v[184:187]\n\t"
    "v_exp_f32 v135, v135\n\t"
    "v_mfma_f32_16x16x32_bf16 v[148:151], a[180:183], a[100:103], v[148:151]\n\t"
    "ds_read_b64_tr_b16 a[238:239], v247 offset:4096\n\t"
    "v_cvt_pk_bf16_f32 v203, v134, v135\n\t"
    "v_mfma_f32_16x16x32_bf16 v[148:151], a[184:187], a[104:107], v[148:151]\n\t"
    "v_exp_f32 v136, v136\n\t"
    "v_mfma_f32_16x16x32_bf16 v[148:151], a[188:191], a[108:111], v[148:151]\n\t"
    "buffer_load_dwordx4 v238, s[68:71], s81 offen nt lds\n\t"
    "v_mfma_f32_16x16x32_bf16 v[152:155], a[160:163], a[112:115], v[188:191]\n\t"
    "v_exp_f32 v137, v137\n\t"
    "v_mfma_f32_16x16x32_bf16 v[152:155], a[164:167], a[116:119], v[152:155]\n\t"
    "ds_read_b128 a[160:163], v224 offset:40960\n\t"
    "v_cvt_pk_bf16_f32 v204, v136, v137\n\t"
    "v_mfma_f32_16x16x32_bf16 v[152:155], a[168:171], a[120:123], v[152:155]\n\t"
    "v_exp_f32 v138, v138\n\t"
    "v_mfma_f32_16x16x32_bf16 v[152:155], a[172:175], a[124:127], v[152:155]\n\t"
    "ds_read_b128 a[164:167], v225 offset:40960\n\t"
    "v_mfma_f32_16x16x32_bf16 v[156:159], a[176:179], a[112:115], v[188:191]\n\t"
    "v_exp_f32 v139, v139\n\t"
    "v_mfma_f32_16x16x32_bf16 v[156:159], a[180:183], a[116:119], v[156:159]\n\t"
    "ds_read_b128 a[168:171], v226 offset:40960\n\t"
    "v_cvt_pk_bf16_f32 v205, v138, v139\n\t"
    "v_mfma_f32_16x16x32_bf16 v[156:159], a[184:187], a[120:123], v[156:159]\n\t"
    "v_exp_f32 v140, v140\n\t"
    "v_mfma_f32_16x16x32_bf16 v[156:159], a[188:191], a[124:127], v[156:159]\n\t"
    "ds_read_b128 a[172:175], v227 offset:40960\n\t"
    "buffer_load_dwordx4 v239, s[68:71], s81 offen offset:1024 nt lds\n\t"
    "s_waitcnt lgkmcnt(2)\n\t"
    "v_mfma_f32_16x16x32_bf16 a[16:19], a[224:227], v[160:163], 0\n\t"
    "v_exp_f32 v141, v141\n\t"
    "v_mfma_f32_16x16x32_bf16 a[48:51], a[224:227], v[168:171], 0\n\t"
    "ds_read_b128 a[176:179], v224 offset:45056\n\t"
    "v_cvt_pk_bf16_f32 v206, v140, v141\n\t"
    "v_mfma_f32_16x16x32_bf16 a[20:23], a[228:231], v[160:163], 0\n\t"
    "v_exp_f32 v142, v142\n\t"
    "v_mfma_f32_16x16x32_bf16 a[52:55], a[228:231], v[168:171], 0\n\t"
    "ds_read_b128 a[180:183], v225 offset:45056\n\t"
    "v_mfma_f32_16x16x32_bf16 a[24:27], a[232:235], v[160:163], 0\n\t"
    "v_exp_f32 v143, v143\n\t"
    "v_mfma_f32_16x16x32_bf16 a[56:59], a[232:235], v[168:171], 0\n\t"
    "ds_read_b128 a[184:187], v226 offset:45056\n\t"
    "v_cvt_pk_bf16_f32 v207, v142, v143\n\t"
    "s_add_u32 m0, s80, 0\n\t"
    "v_mfma_f32_16x16x32_bf16 a[28:31], a[236:239], v[160:163], 0\n\t"
    "v_mfma_f32_16x16x32_bf16 a[60:63], a[236:239], v[168:171], 0\n\t"
    "ds_read_b128 a[188:191], v227 offset:45056\n\t"
    "s_mov_b32 s81, 65536\n\t"
    "v_mfma_f32_16x16x32_bf16 v[64:67], a[192:195], v[208:211], v[64:67]\n\t"
    "v_mfma_f32_16x16x32_bf16 v[96:99], a[192:195], v[212:215], v[96:99]\n\t"
    "ds_read_b64_tr_b16 a[192:193], v228 offset:32768\n\t"
    "buffer_load_dwordx4 v236, s[64:67], s81 offen nt lds\n\t"
    "v_mfma_f32_16x16x32_bf16 v[68:71], a[196:199], v[208:211], v[68:71]\n\t"
    "v_mfma_f32_16x16x32_bf16 v[100:103], a[196:199], v[212:215], v[100:103]\n\t"
    "ds_read_b64_tr_b16 a[194:195], v228 offset:36864\n\t"
    "v_mfma_f32_16x16x32_bf16 v[72:75], a[200:203], v[208:211], v[72:75]\n\t"
    "v_mfma_f32_16x16x32_bf16 v[104:107], a[200:203], v[212:215], v[104:107]\n\t"
    "ds_read_b64_tr_b16 a[196:197], v229 offset:32768\n\t"
    "v_mfma_f32_16x16x32_bf16 v[76:79], a[204:207], v[208:211], v[76:79]\n\t"
    "v_mfma_f32_16x16x32_bf16 v[108:111], a[204:207], v[212:215], v[108:111]\n\t"
    "ds_read_b64_tr_b16 a[198:199], v229 offset:36864\n\t"
    "v_mfma_f32_16x16x32_bf16 v[80:83], a[208:211], v[208:211], v[80:83]\n\t"
    "v_mfma_f32_16x16x32_bf16 v[112:115], a[208:211], v[212:215], v[112:115]\n\t"
    "ds_read_b64_tr_b16 a[200:201], v230 offset:32768\n\t"
    "buffer_load_dwordx4 v237, s[64:67], s81 offen offset:1024 nt lds\n\t"
    "v_mfma_f32_16x16x32_bf16 v[84:87], a[212:215], v[208:211], v[84:87]\n\t"
    "v_mfma_f32_16x16x32_bf16 v[116:119], a[212:215], v[212:215], v[116:119]\n\t"
    "ds_read_b64_tr_b16 a[202:203], v230 offset:36864\n\t"
    "v_mfma_f32_16x16x32_bf16 v[88:91], a[216:219], v[208:211], v[88:91]\n\t"
    "v_mfma_f32_16x16x32_bf16 v[120:123], a[216:219], v[212:215], v[120:123]\n\t"
    "ds_read_b64_tr_b16 a[204:205], v231 offset:32768\n\t"
    "v_mfma_f32_16x16x32_bf16 v[92:95], a[220:223], v[208:211], v[92:95]\n\t"
    "v_mfma_f32_16x16x32_bf16 v[124:127], a[220:223], v[212:215], v[124:127]\n\t"
    "ds_read_b64_tr_b16 a[206:207], v231 offset:36864\n\t"
    "v_mfma_f32_16x16x32_bf16 a[248:251], v[196:199], v[208:211], a[248:251]\n\t"
    "v_exp_f32 v144, v144\n\t"
    "v_mfma_f32_16x16x32_bf16 a[252:255], v[196:199], v[212:215], a[252:255]\n\t"
    "ds_read_b64_tr_b16 a[208:209], v232 offset:32768\n\t"
    "v_mfma_f32_16x16x32_bf16 v[128:131], a[160:163], a[64:67], v[176:179]\n\t"
    "v_exp_f32 v145, v145\n\t"
    "v_mfma_f32_16x16x32_bf16 v[128:131], a[164:167], a[68:71], v[128:131]\n\t"
    "ds_read_b64_tr_b16 a[210:211], v232 offset:36864\n\t"
    "v_cvt_pk_bf16_f32 v208, v144, v145\n\t"
    "s_waitcnt lgkmcnt(2)\n\t"
    "v_mfma_f32_16x16x32_bf16 v[128:131], a[168:171], a[72:75], v[128:131]\n\t"
    "v_exp_f32 v146, v146\n\t"
    "v_mfma_f32_16x16x32_bf16 v[128:131], a[172:175], a[76:79], v[128:131]\n\t"
    "ds_read_b64_tr_b16 a[212:213], v233 offset:32768\n\t"
    "v_mfma_f32_16x16x32_bf16 v[132:135], a[176:179], a[64:67], v[176:179]\n\t"
    "v_exp_f32 v147, v147\n\t"
    "v_mfma_f32_16x16x32_bf16 v[132:135], a[180:183], a[68:71], v[132:135]\n\t"
    "ds_read_b64_tr_b16 a[214:215], v233 offset:36864\n\t"
    "v_cvt_pk_bf16_f32 v209, v146, v147\n\t"
    "v_mfma_f32_16x16x32_bf16 v[132:135], a[184:187], a[72:75], v[132:135]\n\t"
    "v_exp_f32 v148, v148\n\t"
    "v_mfma_f32_16x16x32_bf16 v[132:135], a[188:191], a[76:79], v[132:135]\n\t"
    "ds_read_b64_tr_b16 a[216:217], v234 offset:32768\n\t"
    "v_mfma_f32_16x16x32_bf16 v[136:139], a[160:163], a[80:83], v[180:183]\n\t"
    "v_exp_f32 v149, v149\n\t"
    "v_mfma_f32_16x16x32_bf16 v[136:139], a[164:167], a[84:87], v[136:139]\n\t"
    "ds_read_b64_tr_b16 a[218:219], v234 offset:36864\n\t"
    "v_cvt_pk_bf16_f32 v210, v148, v149\n\t"
    "v_mfma_f32_16x16x32_bf16 v[136:139], a[168:171], a[88:91], v[136:139]\n\t"
    "v_exp_f32 v150, v150\n\t"
    "v_mfma_f32_16x16x32_bf16 v[136:139], a[172:175], a[92:95], v[136:139]\n\t"
    "ds_read_b64_tr_b16 a[220:221], v235 offset:32768\n\t"
    "v_mfma_f32_16x16x32_bf16 v[140:143], a[176:179], a[80:83], v[180:183]\n\t"
    "v_exp_f32 v151, v151\n\t"
    "v_mfma_f32_16x16x32_bf16 v[140:143], a[180:183], a[84:87], v[140:143]\n\t"
    "ds_read_b64_tr_b16 a[222:223], v235 offset:36864\n\t"
    "v_cvt_pk_bf16_f32 v211, v150, v151\n\t"
    "v_mfma_f32_16x16x32_bf16 v[140:143], a[184:187], a[88:91], v[140:143]\n\t"
    "v_exp_f32 v152, v152\n\t"
    "v_mfma_f32_16x16x32_bf16 v[140:143], a[188:191], a[92:95], v[140:143]\n\t"
    "v_mfma_f32_16x16x32_bf16 v[0:3], a[192:195], v[200:203], v[0:3]\n\t"
    "v_exp_f32 v153, v153\n\t"
    "v_mfma_f32_16x16x32_bf16 v[32:35], a[192:195], v[204:207], v[32:35]\n\t"
    "v_cvt_pk_bf16_f32 v212, v152, v153\n\t"
    "v_mfma_f32_16x16x32_bf16 v[4:7], a[196:199], v[200:203], v[4:7]\n\t"
    "v_exp_f32 v154, v154\n\t"
    "v_mfma_f32_16x16x32_bf16 v[36:39], a[196:199], v[204:207], v[36:39]\n\t"
    "v_mfma_f32_16x16x32_bf16 v[8:11], a[200:203], v[200:203], v[8:11]\n\t"
    "v_exp_f32 v155, v155\n\t"
    "v_mfma_f32_16x16x32_bf16 v[40:43], a[200:203], v[204:207], v[40:43]\n\t"
    "v_cvt_pk_bf16_f32 v213, v154, v155\n\t"
    "v_mfma_f32_16x16x32_bf16 v[12:15], a[204:207], v[200:203], v[12:15]\n\t"
    "v_exp_f32 v156, v156\n\t"
    "v_mfma_f32_16x16x32_bf16 v[44:47], a[204:207], v[204:207], v[44:47]\n\t"
    "s_waitcnt lgkmcnt(0)\n\t"
    "v_mfma_f32_16x16x32_bf16 v[16:19], a[208:211], v[200:203], v[16:19]\n\t"
    "v_exp_f32 v157, v157\n\t"
    "v_mfma_f32_16x16x32_bf16 v[48:51], a[208:211], v[204:207], v[48:51]\n\t"
    "v_cvt_pk_bf16_f32 v214, v156, v157\n\t"
    "v_mfma_f32_16x16x32_bf16 v[20:23], a[212:215], v[200:203], v[20:23]\n\t"
    "v_exp_f32 v158, v158\n\t"
    "v_mfma_f32_16x16x32_bf16 v[52:55], a[212:215], v[204:207], v[52:55]\n\t"
    "v_mfma_f32_16x16x32_bf16 v[24:27], a[216:219], v[200:203], v[24:27]\n\t"
    "v_exp_f32 v159, v159\n\t"
    "v_mfma_f32_16x16x32_bf16 v[56:59], a[216:219], v[204:207], v[56:59]\n\t"
    "v_cvt_pk_bf16_f32 v215, v158, v159\n\t"
    "v_mfma_f32_16x16x32_bf16 v[28:31], a[220:223], v[200:203], v[28:31]\n\t"
    "v_mfma_f32_16x16x32_bf16 v[60:63], a[220:223], v[204:207], v[60:63]\n\t"
    "v_mfma_f32_16x16x32_bf16 a[240:243], v[196:199], v[200:203], a[240:243]\n\t"
    "v_exp_f32 v128, v128\n\t"
    "v_mfma_f32_16x16x32_bf16 a[244:247], v[196:199], v[204:207], a[244:247]\n\t"
    "s_waitcnt vmcnt(8)\n\t"
    "s_barrier\n\t"
    "v_mfma_f32_16x16x32_bf16 v[144:147], a[160:163], a[96:99], v[184:187]\n\t"
    "v_exp_f32 v129, v129\n\t"
    "v_mfma_f32_16x16x32_bf16 v[144:147], a[164:167], a[100:103], v[144:147]\n\t"
    "v_cvt_pk_bf16_f32 v200, v128, v129\n\t"
    "s_add_u32 m0, s80, 65536\n\t"
    "v_mfma_f32_16x16x32_bf16 v[144:147], a[168:171], a[104:107], v[144:147]\n\t"
    "v_exp_f32 v130, v130\n\t"
    "v_mfma_f32_16x16x32_bf16 v[144:147], a[172:175], a[108:111], v[144:147]\n\t"
    "s_mov_b32 s81, 65536\n\t"
    "v_mfma_f32_16x16x32_bf16 v[148:151], a[176:179], a[96:99], v[184:187]\n\t"
    "v_exp_f32 v131, v131\n\t"
    "v_mfma_f32_16x16x32_bf16 v[148:151], a[180:183], a[100:103], v[148:151]\n\t"
    "v_cvt_pk_bf16_f32 v201, v130, v131\n\t"
    "v_mfma_f32_16x16x32_bf16 v[148:151], a[184:187], a[104:107], v[148:151]\n\t"
    "v_exp_f32 v132, v132\n\t"
    "v_mfma_f32_16x16x32_bf16 v[148:151], a[188:191], a[108:111], v[148:151]\n\t"
    "buffer_load_dwordx4 v238, s[68:71], s81 offen nt lds\n\t"
    "v_mfma_f32_16x16x32_bf16 v[152:155], a[160:163], a[112:115], v[188:191]\n\t"
    "v_exp_f32 v133, v133\n\t"
    "v_mfma_f32_16x16x32_bf16 v[152:155], a[164:167], a[116:119], v[152:155]\n\t"
    "ds_read_b128 a[160:163], v224 offset:49152\n\t"
    "v_cvt_pk_bf16_f32 v202, v132, v133\n\t"
    "v_mfma_f32_16x16x32_bf16 v[152:155], a[168:171], a[120:123], v[152:155]\n\t"
    "v_exp_f32 v134, v134\n\t"
    "v_mfma_f32_16x16x32_bf16 v[152:155], a[172:175], a[124:127], v[152:155]\n\t"
    "ds_read_b128 a[164:167], v225 offset:49152\n\t"
    "v_mfma_f32_16x16x32_bf16 v[156:159], a[176:179], a[112:115], v[188:191]\n\t"
    "v_exp_f32 v135, v135\n\t"
    "v_mfma_f32_16x16x32_bf16 v[156:159], a[180:183], a[116:119], v[156:159]\n\t"
    "ds_read_b128 a[168:171], v226 offset:49152\n\t"
    "v_cvt_pk_bf16_f32 v203, v134, v135\n\t"
    "v_mfma_f32_16x16x32_bf16 v[156:159], a[184:187], a[120:123], v[156:159]\n\t"
    "v_exp_f32 v136, v136\n\t"
    "v_mfma_f32_16x16x32_bf16 v[156:159], a[188:191], a[124:127], v[156:159]\n\t"
    "ds_read_b128 a[172:175], v227 offset:49152\n\t"
    "buffer_load_dwordx4 v239, s[68:71], s81 offen offset:1024 nt lds\n\t"
    "v_mfma_f32_16x16x32_bf16 v[64:67], a[192:195], v[208:211], v[64:67]\n\t"
    "v_exp_f32 v137, v137\n\t"
    "v_mfma_f32_16x16x32_bf16 v[96:99], a[192:195], v[212:215], v[96:99]\n\t"
    "ds_read_b128 a[176:179], v224 offset:53248\n\t"
    "v_cvt_pk_bf16_f32 v204, v136, v137\n\t"
    "v_mfma_f32_16x16x32_bf16 v[68:71], a[196:199], v[208:211], v[68:71]\n\t"
    "v_exp_f32 v138, v138\n\t"
    "v_mfma_f32_16x16x32_bf16 v[100:103], a[196:199], v[212:215], v[100:103]\n\t"
    "ds_read_b128 a[180:183], v225 offset:53248\n\t"
    "v_mfma_f32_16x16x32_bf16 v[72:75], a[200:203], v[208:211], v[72:75]\n\t"
    "v_exp_f32 v139, v139\n\t"
    "v_mfma_f32_16x16x32_bf16 v[104:107], a[200:203], v[212:215], v[104:107]\n\t"
    "ds_read_b128 a[184:187], v226 offset:53248\n\t"
    "v_cvt_pk_bf16_f32 v205, v138, v139\n\t"
    "s_add_u32 m0, s80, 8192\n\t"
    "v_mfma_f32_16x16x32_bf16 v[76:79], a[204:207], v[208:211], v[76:79]\n\t"
    "v_exp_f32 v140, v140\n\t"
    "v_mfma_f32_16x16x32_bf16 v[108:111], a[204:207], v[212:215], v[108:111]\n\t"
    "ds_read_b128 a[188:191], v227 offset:53248\n\t"
    "s_mov_b32 s81, 73728\n\t"
    "v_mfma_f32_16x16x32_bf16 v[80:83], a[208:211], v[208:211], v[80:83]\n\t"
    "v_exp_f32 v141, v141\n\t"
    "v_mfma_f32_16x16x32_bf16 v[112:115], a[208:211], v[212:215], v[112:115]\n\t"
    "ds_read_b64_tr_b16 a[192:193], v228 offset:40960\n\t"
    "v_cvt_pk_bf16_f32 v206, v140, v141\n\t"
    "v_mfma_f32_16x16x32_bf16 v[84:87], a[212:215], v[208:211], v[84:87]\n\t"
    "v_exp_f32 v142, v142\n\t"
    "v_mfma_f32_16x16x32_bf16 v[116:119], a[212:215], v[212:215], v[116:119]\n\t"
    "ds_read_b64_tr_b16 a[194:195], v228 offset:45056\n\t"
    "buffer_load_dwordx4 v236, s[64:67], s81 offen nt lds\n\t"
    "v_mfma_f32_16x16x32_bf16 v[88:91], a[216:219], v[208:211], v[88:91]\n\t"
    "v_exp_f32 v143, v143\n\t"
    "v_mfma_f32_16x16x32_bf16 v[120:123], a[216:219], v[212:215], v[120:123]\n\t"
    "ds_read_b64_tr_b16 a[196:197], v229 offset:40960\n\t"
    "v_cvt_pk_bf16_f32 v207, v142, v143\n\t"
    "v_mfma_f32_16x16x32_bf16 v[92:95], a[220:223], v[208:211], v[92:95]\n\t"
    "v_mfma_f32_16x16x32_bf16 v[124:127], a[220:223], v[212:215], v[124:127]\n\t"
    "ds_read_b64_tr_b16 a[198:199], v229 offset:45056\n\t"
    "v_mfma_f32_16x16x32_bf16 a[248:251], v[196:199], v[208:211], a[248:251]\n\t"
    "v_exp_f32 v144, v144\n\t"
    "v_mfma_f32_16x16x32_bf16 a[252:255], v[196:199], v[212:215], a[252:255]\n\t"
    "ds_read_b64_tr_b16 a[200:201], v230 offset:40960\n\t"
    "s_waitcnt lgkmcnt(2)\n\t"
    "v_mfma_f32_16x16x32_bf16 v[128:131], a[160:163], a[64:67], v[176:179]\n\t"
    "v_exp_f32 v145, v145\n\t"
    "v_mfma_f32_16x16x32_bf16 v[128:131], a[164:167], a[68:71], v[128:131]\n\t"
    "ds_read_b64_tr_b16 a[202:203], v230 offset:45056\n\t"
    "v_cvt_pk_bf16_f32 v208, v144, v145\n\t"
    "v_mfma_f32_16x16x32_bf16 v[128:131], a[168:171], a[72:75], v[128:131]\n\t"
    "v_exp_f32 v146, v146\n\t"
    "v_mfma_f32_16x16x32_bf16 v[128:131], a[172:175], a[76:79], v[128:131]\n\t"
    "ds_read_b64_tr_b16 a[204:205], v231 offset:40960\n\t"
    "buffer_load_dwordx4 v237, s[64:67], s81 offen offset:1024 nt lds\n\t"
    "v_mfma_f32_16x16x32_bf16 v[132:135], a[176:179], a[64:67], v[176:179]\n\t"
    "v_exp_f32 v147, v147\n\t"
    "v_mfma_f32_16x16x32_bf16 v[132:135], a[180:183], a[68:71], v[132:135]\n\t"
    "ds_read_b64_tr_b16 a[206:207], v231 offset:45056\n\t"
    "v_cvt_pk_bf16_f32 v209, v146, v147\n\t"
    "v_mfma_f32_16x16x32_bf16 v[132:135], a[184:187], a[72:75], v[132:135]\n\t"
    "v_exp_f32 v148, v148\n\t"
    "v_mfma_f32_16x16x32_bf16 v[132:135], a[188:191], a[76:79], v[132:135]\n\t"
    "ds_read_b64_tr_b16 a[208:209], v232 offset:40960\n\t"
    "v_mfma_f32_16x16x32_bf16 v[136:139], a[160:163], a[80:83], v[180:183]\n\t"
    "v_exp_f32 v149, v149\n\t"
    "v_mfma_f32_16x16x32_bf16 v[136:139], a[164:167], a[84:87], v[136:139]\n\t"
    "ds_read_b64_tr_b16 a[210:211], v232 offset:45056\n\t"
    "v_cvt_pk_bf16_f32 v210, v148, v149\n\t"
    "v_mfma_f32_16x16x32_bf16 v[136:139], a[168:171], a[88:91], v[136:139]\n\t"
    "v_exp_f32 v150, v150\n\t"
    "v_mfma_f32_16x16x32_bf16 v[136:139], a[172:175], a[92:95], v[136:139]\n\t"
    "ds_read_b64_tr_b16 a[212:213], v233 offset:40960\n\t"
    "v_mfma_f32_16x16x32_bf16 v[140:143], a[176:179], a[80:83], v[180:183]\n\t"
    "v_exp_f32 v151, v151\n\t"
    "v_mfma_f32_16x16x32_bf16 v[140:143], a[180:183], a[84:87], v[140:143]\n\t"
    "ds_read_b64_tr_b16 a[214:215], v233 offset:45056\n\t"
    "v_cvt_pk_bf16_f32 v211, v150, v151\n\t"
    "v_mfma_f32_16x16x32_bf16 v[140:143], a[184:187], a[88:91], v[140:143]\n\t"
    "v_exp_f32 v152, v152\n\t"
    "v_mfma_f32_16x16x32_bf16 v[140:143], a[188:191], a[92:95], v[140:143]\n\t"
    "ds_read_b64_tr_b16 a[216:217], v234 offset:40960\n\t"
    "v_mfma_f32_16x16x32_bf16 v[0:3], a[192:195], v[200:203], v[0:3]\n\t"
    "v_exp_f32 v153, v153\n\t"
    "v_mfma_f32_16x16x32_bf16 v[32:35], a[192:195], v[204:207], v[32:35]\n\t"
    "ds_read_b64_tr_b16 a[218:219], v234 offset:45056\n\t"
    "v_cvt_pk_bf16_f32 v212, v152, v153\n\t"
    "s_waitcnt lgkmcnt(2)\n\t"
    "v_mfma_f32_16x16x32_bf16 v[4:7], a[196:199], v[200:203], v[4:7]\n\t"
    "v_exp_f32 v154, v154\n\t"
    "v_mfma_f32_16x16x32_bf16 v[36:39], a[196:199], v[204:207], v[36:39]\n\t"
    "ds_read_b64_tr_b16 a[220:221], v235 offset:40960\n\t"
    "v_mfma_f32_16x16x32_bf16 v[8:11], a[200:203], v[200:203], v[8:11]\n\t"
    "v_exp_f32 v155, v155\n\t"
    "v_mfma_f32_16x16x32_bf16 v[40:43], a[200:203], v[204:207], v[40:43]\n\t"
    "ds_read_b64_tr_b16 a[222:223], v235 offset:45056\n\t"
    "v_cvt_pk_bf16_f32 v213, v154, v155\n\t"
    "v_mfma_f32_16x16x32_bf16 v[12:15], a[204:207], v[200:203], v[12:15]\n\t"
    "v_exp_f32 v156, v156\n\t"
    "v_mfma_f32_16x16x32_bf16 v[44:47], a[204:207], v[204:207], v[44:47]\n\t"
    "v_mfma_f32_16x16x32_bf16 v[16:19], a[208:211], v[200:203], v[16:19]\n\t"
    "v_exp_f32 v157, v157\n\t"
    "v_mfma_f32_16x16x32_bf16 v[48:51], a[208:211], v[204:207], v[48:51]\n\t"
    "v_cvt_pk_bf16_f32 v214, v156, v157\n\t"
    "v_mfma_f32_16x16x32_bf16 v[20:23], a[212:215], v[200:203], v[20:23]\n\t"
    "v_exp_f32 v158, v158\n\t"
    "v_mfma_f32_16x16x32_bf16 v[52:55], a[212:215], v[204:207], v[52:55]\n\t"
    "s_waitcnt lgkmcnt(0)\n\t"
    "v_mfma_f32_16x16x32_bf16 v[24:27], a[216:219], v[200:203], v[24:27]\n\t"
    "v_exp_f32 v159, v159\n\t"
    "v_mfma_f32_16x16x32_bf16 v[56:59], a[216:219], v[204:207], v[56:59]\n\t"
    "v_cvt_pk_bf16_f32 v215, v158, v159\n\t"
    "v_mfma_f32_16x16x32_bf16 v[28:31], a[220:223], v[200:203], v[28:31]\n\t"
    "v_mfma_f32_16x16x32_bf16 v[60:63], a[220:223], v[204:207], v[60:63]\n\t"
    "v_mfma_f32_16x16x32_bf16 a[240:243], v[196:199], v[200:203], a[240:243]\n\t"
    "v_exp_f32 v128, v128\n\t"
    "v_mfma_f32_16x16x32_bf16 a[244:247], v[196:199], v[204:207], a[244:247]\n\t"
    "s_waitcnt vmcnt(8)\n\t"
    "s_barrier\n\t"
    "v_mfma_f32_16x16x32_bf16 v[144:147], a[160:163], a[96:99], v[184:187]\n\t"
    "v_exp_f32 v129, v129\n\t"
    "v_mfma_f32_16x16x32_bf16 v[144:147], a[164:167], a[100:103], v[144:147]\n\t"
    "v_cvt_pk_bf16_f32 v200, v128, v129\n\t"
    "s_add_u32 m0, s80, 73728\n\t"
    "v_mfma_f32_16x16x32_bf16 v[144:147], a[168:171], a[104:107], v[144:147]\n\t"
    "v_exp_f32 v130, v130\n\t"
    "v_mfma_f32_16x16x32_bf16 v[144:147], a[172:175], a[108:111], v[144:147]\n\t"
    "s_mov_b32 s81, 73728\n\t"
    "v_mfma_f32_16x16x32_bf16 v[148:151], a[176:179], a[96:99], v[184:187]\n\t"
    "v_exp_f32 v131, v131\n\t"
    "v_mfma_f32_16x16x32_bf16 v[148:151], a[180:183], a[100:103], v[148:151]\n\t"
    "v_cvt_pk_bf16_f32 v201, v130, v131\n\t"
    "v_mfma_f32_16x16x32_bf16 v[148:151], a[184:187], a[104:107], v[148:151]\n\t"
    "v_exp_f32 v132, v132\n\t"
    "v_mfma_f32_16x16x32_bf16 v[148:151], a[188:191], a[108:111], v[148:151]\n\t"
    "buffer_load_dwordx4 v238, s[68:71], s81 offen nt lds\n\t"
    "v_mfma_f32_16x16x32_bf16 v[152:155], a[160:163], a[112:115], v[188:191]\n\t"
    "v_exp_f32 v133, v133\n\t"
    "v_mfma_f32_16x16x32_bf16 v[152:155], a[164:167], a[116:119], v[152:155]\n\t"
    "ds_read_b128 a[160:163], v224 offset:57344\n\t"
    "v_cvt_pk_bf16_f32 v202, v132, v133\n\t"
    "v_mfma_f32_16x16x32_bf16 v[152:155], a[168:171], a[120:123], v[152:155]\n\t"
    "v_exp_f32 v134, v134\n\t"
    "v_mfma_f32_16x16x32_bf16 v[152:155], a[172:175], a[124:127], v[152:155]\n\t"
    "ds_read_b128 a[164:167], v225 offset:57344\n\t"
    "v_mfma_f32_16x16x32_bf16 v[156:159], a[176:179], a[112:115], v[188:191]\n\t"
    "v_exp_f32 v135, v135\n\t"
    "v_mfma_f32_16x16x32_bf16 v[156:159], a[180:183], a[116:119], v[156:159]\n\t"
    "ds_read_b128 a[168:171], v226 offset:57344\n\t"
    "v_cvt_pk_bf16_f32 v203, v134, v135\n\t"
    "v_mfma_f32_16x16x32_bf16 v[156:159], a[184:187], a[120:123], v[156:159]\n\t"
    "v_exp_f32 v136, v136\n\t"
    "v_mfma_f32_16x16x32_bf16 v[156:159], a[188:191], a[124:127], v[156:159]\n\t"
    "ds_read_b128 a[172:175], v227 offset:57344\n\t"
    "buffer_load_dwordx4 v239, s[68:71], s81 offen offset:1024 nt lds\n\t"
    "v_mfma_f32_16x16x32_bf16 v[64:67], a[192:195], v[208:211], v[64:67]\n\t"
    "v_exp_f32 v137, v137\n\t"
    "v_mfma_f32_16x16x32_bf16 v[96:99], a[192:195], v[212:215], v[96:99]\n\t"
    "ds_read_b128 a[176:179], v224 offset:61440\n\t"
    "v_cvt_pk_bf16_f32 v204, v136, v137\n\t"
    "v_mfma_f32_16x16x32_bf16 v[68:71], a[196:199], v[208:211], v[68:71]\n\t"
    "v_exp_f32 v138, v138\n\t"
    "v_mfma_f32_16x16x32_bf16 v[100:103], a[196:199], v[212:215], v[100:103]\n\t"
    "ds_read_b128 a[180:183], v225 offset:61440\n\t"
    "v_mfma_f32_16x16x32_bf16 v[72:75], a[200:203], v[208:211], v[72:75]\n\t"
    "v_exp_f32 v139, v139\n\t"
    "v_mfma_f32_16x16x32_bf16 v[104:107], a[200:203], v[212:215], v[104:107]\n\t"
    "ds_read_b128 a[184:187], v226 offset:61440\n\t"
    "v_cvt_pk_bf16_f32 v205, v138, v139\n\t"
    "s_add_u32 m0, s80, 16384\n\t"
    "v_mfma_f32_16x16x32_bf16 v[76:79], a[204:207], v[208:211], v[76:79]\n\t"
    "v_exp_f32 v140, v140\n\t"
    "v_mfma_f32_16x16x32_bf16 v[108:111], a[204:207], v[212:215], v[108:111]\n\t"
    "ds_read_b128 a[188:191], v227 offset:61440\n\t"
    "s_mov_b32 s81, 81920\n\t"
    "v_mfma_f32_16x16x32_bf16 v[80:83], a[208:211], v[208:211], v[80:83]\n\t"
    "v_exp_f32 v141, v141\n\t"
    "v_mfma_f32_16x16x32_bf16 v[112:115], a[208:211], v[212:215], v[112:115]\n\t"
    "ds_read_b64_tr_b16 a[192:193], v228 offset:49152\n\t"
    "v_cvt_pk_bf16_f32 v206, v140, v141\n\t"
    "v_mfma_f32_16x16x32_bf16 v[84:87], a[212:215], v[208:211], v[84:87]\n\t"
    "v_exp_f32 v142, v142\n\t"
    "v_mfma_f32_16x16x32_bf16 v[116:119], a[212:215], v[212:215], v[116:119]\n\t"
    "ds_read_b64_tr_b16 a[194:195], v228 offset:53248\n\t"
    "buffer_load_dwordx4 v236, s[64:67], s81 offen nt lds\n\t"
    "v_mfma_f32_16x16x32_bf16 v[88:91], a[216:219], v[208:211], v[88:91]\n\t"
    "v_exp_f32 v143, v143\n\t"
    "v_mfma_f32_16x16x32_bf16 v[120:123], a[216:219], v[212:215], v[120:123]\n\t"
    "ds_read_b64_tr_b16 a[196:197], v229 offset:49152\n\t"
    "v_cvt_pk_bf16_f32 v207, v142, v143\n\t"
    "v_mfma_f32_16x16x32_bf16 v[92:95], a[220:223], v[208:211], v[92:95]\n\t"
    "v_mfma_f32_16x16x32_bf16 v[124:127], a[220:223], v[212:215], v[124:127]\n\t"
    "ds_read_b64_tr_b16 a[198:199], v229 offset:53248\n\t"
    "v_mfma_f32_16x16x32_bf16 a[248:251], v[196:199], v[208:211], a[248:251]\n\t"
    "v_exp_f32 v144, v144\n\t"
    "v_mfma_f32_16x16x32_bf16 a[252:255], v[196:199], v[212:215], a[252:255]\n\t"
    "ds_read_b64_tr_b16 a[200:201], v230 offset:49152\n\t"
    "s_waitcnt lgkmcnt(2)\n\t"
    "v_mfma_f32_16x16x32_bf16 v[128:131], a[160:163], a[64:67], v[176:179]\n\t"
    "v_exp_f32 v145, v145\n\t"
    "v_mfma_f32_16x16x32_bf16 v[128:131], a[164:167], a[68:71], v[128:131]\n\t"
    "ds_read_b64_tr_b16 a[202:203], v230 offset:53248\n\t"
    "v_cvt_pk_bf16_f32 v208, v144, v145\n\t"
    "v_mfma_f32_16x16x32_bf16 v[128:131], a[168:171], a[72:75], v[128:131]\n\t"
    "v_exp_f32 v146, v146\n\t"
    "v_mfma_f32_16x16x32_bf16 v[128:131], a[172:175], a[76:79], v[128:131]\n\t"
    "ds_read_b64_tr_b16 a[204:205], v231 offset:49152\n\t"
    "buffer_load_dwordx4 v237, s[64:67], s81 offen offset:1024 nt lds\n\t"
    "v_mfma_f32_16x16x32_bf16 v[132:135], a[176:179], a[64:67], v[176:179]\n\t"
    "v_exp_f32 v147, v147\n\t"
    "v_mfma_f32_16x16x32_bf16 v[132:135], a[180:183], a[68:71], v[132:135]\n\t"
    "ds_read_b64_tr_b16 a[206:207], v231 offset:53248\n\t"
    "v_cvt_pk_bf16_f32 v209, v146, v147\n\t"
    "v_mfma_f32_16x16x32_bf16 v[132:135], a[184:187], a[72:75], v[132:135]\n\t"
    "v_exp_f32 v148, v148\n\t"
    "v_mfma_f32_16x16x32_bf16 v[132:135], a[188:191], a[76:79], v[132:135]\n\t"
    "ds_read_b64_tr_b16 a[208:209], v232 offset:49152\n\t"
    "v_mfma_f32_16x16x32_bf16 v[136:139], a[160:163], a[80:83], v[180:183]\n\t"
    "v_exp_f32 v149, v149\n\t"
    "v_mfma_f32_16x16x32_bf16 v[136:139], a[164:167], a[84:87], v[136:139]\n\t"
    "ds_read_b64_tr_b16 a[210:211], v232 offset:53248\n\t"
    "v_cvt_pk_bf16_f32 v210, v148, v149\n\t"
    "v_mfma_f32_16x16x32_bf16 v[136:139], a[168:171], a[88:91], v[136:139]\n\t"
    "v_exp_f32 v150, v150\n\t"
    "v_mfma_f32_16x16x32_bf16 v[136:139], a[172:175], a[92:95], v[136:139]\n\t"
    "ds_read_b64_tr_b16 a[212:213], v233 offset:49152\n\t"
    "v_mfma_f32_16x16x32_bf16 v[140:143], a[176:179], a[80:83], v[180:183]\n\t"
    "v_exp_f32 v151, v151\n\t"
    "v_mfma_f32_16x16x32_bf16 v[140:143], a[180:183], a[84:87], v[140:143]\n\t"
    "ds_read_b64_tr_b16 a[214:215], v233 offset:53248\n\t"
    "v_cvt_pk_bf16_f32 v211, v150, v151\n\t"
    "v_mfma_f32_16x16x32_bf16 v[140:143], a[184:187], a[88:91], v[140:143]\n\t"
    "v_exp_f32 v152, v152\n\t"
    "v_mfma_f32_16x16x32_bf16 v[140:143], a[188:191], a[92:95], v[140:143]\n\t"
    "ds_read_b64_tr_b16 a[216:217], v234 offset:49152\n\t"
    "v_mfma_f32_16x16x32_bf16 v[0:3], a[192:195], v[200:203], v[0:3]\n\t"
    "v_exp_f32 v153, v153\n\t"
    "v_mfma_f32_16x16x32_bf16 v[32:35], a[192:195], v[204:207], v[32:35]\n\t"
    "ds_read_b64_tr_b16 a[218:219], v234 offset:53248\n\t"
    "v_cvt_pk_bf16_f32 v212, v152, v153\n\t"
    "s_waitcnt lgkmcnt(2)\n\t"
    "v_mfma_f32_16x16x32_bf16 v[4:7], a[196:199], v[200:203], v[4:7]\n\t"
    "v_exp_f32 v154, v154\n\t"
    "v_mfma_f32_16x16x32_bf16 v[36:39], a[196:199], v[204:207], v[36:39]\n\t"
    "ds_read_b64_tr_b16 a[220:221], v235 offset:49152\n\t"
    "v_mfma_f32_16x16x32_bf16 v[8:11], a[200:203], v[200:203], v[8:11]\n\t"
    "v_exp_f32 v155, v155\n\t"
    "v_mfma_f32_16x16x32_bf16 v[40:43], a[200:203], v[204:207], v[40:43]\n\t"
    "ds_read_b64_tr_b16 a[222:223], v235 offset:53248\n\t"
    "v_cvt_pk_bf16_f32 v213, v154, v155\n\t"
    "v_mfma_f32_16x16x32_bf16 v[12:15], a[204:207], v[200:203], v[12:15]\n\t"
    "v_exp_f32 v156, v156\n\t"
    "v_mfma_f32_16x16x32_bf16 v[44:47], a[204:207], v[204:207], v[44:47]\n\t"
    "v_add_u32 v240, s63, v224\n\t"
    "ds_read_b128 a[224:227], v240 offset:32768\n\t"
    "v_mfma_f32_16x16x32_bf16 v[16:19], a[208:211], v[200:203], v[16:19]\n\t"
    "v_exp_f32 v157, v157\n\t"
    "v_mfma_f32_16x16x32_bf16 v[48:51], a[208:211], v[204:207], v[48:51]\n\t"
    "v_add_u32 v241, s63, v225\n\t"
    "ds_read_b128 a[228:231], v241 offset:32768\n\t"
    "v_cvt_pk_bf16_f32 v214, v156, v157\n\t"
    "v_mfma_f32_16x16x32_bf16 v[20:23], a[212:215], v[200:203], v[20:23]\n\t"
    "v_exp_f32 v158, v158\n\t"
    "v_mfma_f32_16x16x32_bf16 v[52:55], a[212:215], v[204:207], v[52:55]\n\t"
    "v_add_u32 v242, s63, v226\n\t"
    "ds_read_b128 a[232:235], v242 offset:32768\n\t"
    "s_waitcnt lgkmcnt(2)\n\t"
    "v_mfma_f32_16x16x32_bf16 v[24:27], a[216:219], v[200:203], v[24:27]\n\t"
    "v_exp_f32 v159, v159\n\t"
    "v_mfma_f32_16x16x32_bf16 v[56:59], a[216:219], v[204:207], v[56:59]\n\t"
    "v_add_u32 v243, s63, v227\n\t"
    "ds_read_b128 a[236:239], v243 offset:32768\n\t"
    "v_cvt_pk_bf16_f32 v215, v158, v159\n\t"
    "v_mfma_f32_16x16x32_bf16 v[28:31], a[220:223], v[200:203], v[28:31]\n\t"
    "v_mfma_f32_16x16x32_bf16 v[60:63], a[220:223], v[204:207], v[60:63]\n\t"
    "v_mfma_f32_16x16x32_bf16 a[240:243], v[196:199], v[200:203], a[240:243]\n\t"
    "v_exp_f32 v128, v128\n\t"
    "v_mfma_f32_16x16x32_bf16 a[244:247], v[196:199], v[204:207], a[244:247]\n\t"
    "v_mfma_f32_16x16x32_bf16 v[160:163], a[224:227], a[128:131], 0\n\t"
    "v_exp_f32 v129, v129\n\t"
    "s_waitcnt lgkmcnt(0)\n\t"
    "v_mfma_f32_16x16x32_bf16 v[160:163], a[228:231], a[132:135], v[160:163]\n\t"
    "v_cvt_pk_bf16_f32 v200, v128, v129\n\t"
    "v_mfma_f32_16x16x32_bf16 v[160:163], a[232:235], a[136:139], v[160:163]\n\t"
    "v_exp_f32 v130, v130\n\t"
    "v_mfma_f32_16x16x32_bf16 v[160:163], a[236:239], a[140:143], v[160:163]\n\t"
    "v_mfma_f32_16x16x32_bf16 v[168:171], a[224:227], a[144:147], 0\n\t"
    "v_exp_f32 v131, v131\n\t"
    "v_mfma_f32_16x16x32_bf16 v[168:171], a[228:231], a[148:151], v[168:171]\n\t"
    "ds_read_b128 a[224:227], v240 offset:36864\n\t"
    "v_cvt_pk_bf16_f32 v201, v130, v131\n\t"
    "v_mfma_f32_16x16x32_bf16 v[168:171], a[232:235], a[152:155], v[168:171]\n\t"
    "v_exp_f32 v132, v132\n\t"
    "v_mfma_f32_16x16x32_bf16 v[168:171], a[236:239], a[156:159], v[168:171]\n\t"
    "ds_read_b128 a[228:231], v241 offset:36864\n\t"
    "s_waitcnt vmcnt(8)\n\t"
    "s_barrier\n\t"
    "v_mfma_f32_16x16x32_bf16 v[144:147], a[160:163], a[96:99], v[184:187]\n\t"
    "v_exp_f32 v133, v133\n\t"
    "v_mfma_f32_16x16x32_bf16 v[144:147], a[164:167], a[100:103], v[144:147]\n\t"
    "ds_read_b128 a[232:235], v242 offset:36864\n\t"
    "v_cvt_pk_bf16_f32 v202, v132, v133\n\t"
    "s_add_u32 m0, s80, 81920\n\t"
    "v_mfma_f32_16x16x32_bf16 v[144:147], a[168:171], a[104:107], v[144:147]\n\t"
    "v_exp_f32 v134, v134\n\t"
    "v_mfma_f32_16x16x32_bf16 v[144:147], a[172:175], a[108:111], v[144:147]\n\t"
    "ds_read_b128 a[236:239], v243 offset:36864\n\t"
    "s_mov_b32 s81, 81920\n\t"
    "v_mfma_f32_16x16x32_bf16 v[148:151], a[176:179], a[96:99], v[184:187]\n\t"
    "v_exp_f32 v135, v135\n\t"
    "v_mfma_f32_16x16x32_bf16 v[148:151], a[180:183], a[100:103], v[148:151]\n\t"
    "v_cvt_pk_bf16_f32 v203, v134, v135\n\t"
    "v_mfma_f32_16x16x32_bf16 v[148:151], a[184:187], a[104:107], v[148:151]\n\t"
    "v_exp_f32 v136, v136\n\t"
    "v_mfma_f32_16x16x32_bf16 v[148:151], a[188:191], a[108:111], v[148:151]\n\t"
    "buffer_load_dwordx4 v238, s[68:71], s81 offen nt lds\n\t"
    "v_mfma_f32_16x16x32_bf16 v[152:155], a[160:163], a[112:115], v[188:191]\n\t"
    "v_exp_f32 v137, v137\n\t"
    "v_mfma_f32_16x16x32_bf16 v[152:155], a[164:167], a[116:119], v[152:155]\n\t"
    "ds_read_b128 a[160:163], v224 offset:0\n\t"
    "v_cvt_pk_bf16_f32 v204, v136, v137\n\t"
    "v_mfma_f32_16x16x32_bf16 v[152:155], a[168:171], a[120:123], v[152:155]\n\t"
    "v_exp_f32 v138, v138\n\t"
    "v_mfma_f32_16x16x32_bf16 v[152:155], a[172:175], a[124:127], v[152:155]\n\t"
    "ds_read_b128 a[164:167], v225 offset:0\n\t"
    "v_mfma_f32_16x16x32_bf16 v[156:159], a[176:179], a[112:115], v[188:191]\n\t"
    "v_exp_f32 v139, v139\n\t"
    "v_mfma_f32_16x16x32_bf16 v[156:159], a[180:183], a[116:119], v[156:159]\n\t"
    "ds_read_b128 a[168:171], v226 offset:0\n\t"
    "v_cvt_pk_bf16_f32 v205, v138, v139\n\t"
    "v_mfma_f32_16x16x32_bf16 v[156:159], a[184:187], a[120:123], v[156:159]\n\t"
    "v_exp_f32 v140, v140\n\t"
    "v_mfma_f32_16x16x32_bf16 v[156:159], a[188:191], a[124:127], v[156:159]\n\t"
    "ds_read_b128 a[172:175], v227 offset:0\n\t"
    "buffer_load_dwordx4 v239, s[68:71], s81 offen offset:1024 nt lds\n\t"
    "s_waitcnt lgkmcnt(2)\n\t"
    "v_mfma_f32_16x16x32_bf16 v[164:167], a[224:227], a[128:131], 0\n\t"
    "v_exp_f32 v141, v141\n\t"
    "v_mfma_f32_16x16x32_bf16 v[164:167], a[228:231], a[132:135], v[164:167]\n\t"
    "ds_read_b128 a[176:179], v224 offset:4096\n\t"
    "v_cvt_pk_bf16_f32 v206, v140, v141\n\t"
    "v_mfma_f32_16x16x32_bf16 v[164:167], a[232:235], a[136:139], v[164:167]\n\t"
    "v_exp_f32 v142, v142\n\t"
    "v_mfma_f32_16x16x32_bf16 v[164:167], a[236:239], a[140:143], v[164:167]\n\t"
    "ds_read_b128 a[180:183], v225 offset:4096\n\t"
    "v_mfma_f32_16x16x32_bf16 v[172:175], a[224:227], a[144:147], 0\n\t"
    "v_exp_f32 v143, v143\n\t"
    "v_mfma_f32_16x16x32_bf16 v[172:175], a[228:231], a[148:151], v[172:175]\n\t"
    "ds_read_b128 a[184:187], v226 offset:4096\n\t"
    "v_cvt_pk_bf16_f32 v207, v142, v143\n\t"
    "s_add_u32 m0, s80, 24576\n\t"
    "v_mfma_f32_16x16x32_bf16 v[172:175], a[232:235], a[152:155], v[172:175]\n\t"
    "v_mfma_f32_16x16x32_bf16 v[172:175], a[236:239], a[156:159], v[172:175]\n\t"
    "ds_read_b128 a[188:191], v227 offset:4096\n\t"
    "s_mov_b32 s81, 90112\n\t"
    "v_mfma_f32_16x16x32_bf16 v[64:67], a[192:195], v[208:211], v[64:67]\n\t"
    "v_mfma_f32_16x16x32_bf16 v[96:99], a[192:195], v[212:215], v[96:99]\n\t"
    "ds_read_b64_tr_b16 a[192:193], v228 offset:57344\n\t"
    "buffer_load_dwordx4 v236, s[64:67], s81 offen nt lds\n\t"
    "v_mfma_f32_16x16x32_bf16 v[68:71], a[196:199], v[208:211], v[68:71]\n\t"
    "v_mfma_f32_16x16x32_bf16 v[100:103], a[196:199], v[212:215], v[100:103]\n\t"
    "ds_read_b64_tr_b16 a[194:195], v228 offset:61440\n\t"
    "v_mfma_f32_16x16x32_bf16 v[72:75], a[200:203], v[208:211], v[72:75]\n\t"
    "v_mfma_f32_16x16x32_bf16 v[104:107], a[200:203], v[212:215], v[104:107]\n\t"
    "ds_read_b64_tr_b16 a[196:197], v229 offset:57344\n\t"
    "v_mfma_f32_16x16x32_bf16 v[76:79], a[204:207], v[208:211], v[76:79]\n\t"
    "v_mfma_f32_16x16x32_bf16 v[108:111], a[204:207], v[212:215], v[108:111]\n\t"
    "ds_read_b64_tr_b16 a[198:199], v229 offset:61440\n\t"
    "v_mfma_f32_16x16x32_bf16 v[80:83], a[208:211], v[208:211], v[80:83]\n\t"
    "v_mfma_f32_16x16x32_bf16 v[112:115], a[208:211], v[212:215], v[112:115]\n\t"
    "ds_read_b64_tr_b16 a[200:201], v230 offset:57344\n\t"
    "buffer_load_dwordx4 v237, s[64:67], s81 offen offset:1024 nt lds\n\t"
    "v_mfma_f32_16x16x32_bf16 v[84:87], a[212:215], v[208:211], v[84:87]\n\t"
    "v_mfma_f32_16x16x32_bf16 v[116:119], a[212:215], v[212:215], v[116:119]\n\t"
    "ds_read_b64_tr_b16 a[202:203], v230 offset:61440\n\t"
    "v_mfma_f32_16x16x32_bf16 v[88:91], a[216:219], v[208:211], v[88:91]\n\t"
    "v_mfma_f32_16x16x32_bf16 v[120:123], a[216:219], v[212:215], v[120:123]\n\t"
    "ds_read_b64_tr_b16 a[204:205], v231 offset:57344\n\t"
    "v_mfma_f32_16x16x32_bf16 v[92:95], a[220:223], v[208:211], v[92:95]\n\t"
    "v_mfma_f32_16x16x32_bf16 v[124:127], a[220:223], v[212:215], v[124:127]\n\t"
    "ds_read_b64_tr_b16 a[206:207], v231 offset:61440\n\t"
    "v_mfma_f32_16x16x32_bf16 a[248:251], v[196:199], v[208:211], a[248:251]\n\t"
    "v_exp_f32 v144, v144\n\t"
    "v_mfma_f32_16x16x32_bf16 a[252:255], v[196:199], v[212:215], a[252:255]\n\t"
    "ds_read_b64_tr_b16 a[208:209], v232 offset:57344\n\t"
    "v_exp_f32 v145, v145\n\t"
    "v_mfma_f32_16x16x32_bf16 v[128:131], a[160:163], a[64:67], v[176:179]\n\t"
    "v_exp_f32 v146, v146\n\t"
    "v_cvt_pk_bf16_f32 v208, v144, v145\n\t"
    "v_mfma_f32_16x16x32_bf16 v[128:131], a[164:167], a[68:71], v[128:131]\n\t"
    "ds_read_b64_tr_b16 a[210:211], v232 offset:61440\n\t"
    "v_exp_f32 v147, v147\n\t"
    "s_waitcnt lgkmcnt(2)\n\t"
    "v_mfma_f32_16x16x32_bf16 v[128:131], a[168:171], a[72:75], v[128:131]\n\t"
    "v_exp_f32 v148, v148\n\t"
    "v_cvt_pk_bf16_f32 v209, v146, v147\n\t"
    "v_mfma_f32_16x16x32_bf16 v[128:131], a[172:175], a[76:79], v[128:131]\n\t"
    "ds_read_b64_tr_b16 a[212:213], v233 offset:57344\n\t"
    "v_exp_f32 v149, v149\n\t"
    "v_mfma_f32_16x16x32_bf16 v[132:135], a[176:179], a[64:67], v[176:179]\n\t"
    "v_exp_f32 v150, v150\n\t"
    "v_cvt_pk_bf16_f32 v210, v148, v149\n\t"
    "v_mfma_f32_16x16x32_bf16 v[132:135], a[180:183], a[68:71], v[132:135]\n\t"
    "ds_read_b64_tr_b16 a[214:215], v233 offset:61440\n\t"
    "v_exp_f32 v151, v151\n\t"
    "v_mfma_f32_16x16x32_bf16 v[132:135], a[184:187], a[72:75], v[132:135]\n\t"
    "v_exp_f32 v152, v152\n\t"
    "v_cvt_pk_bf16_f32 v211, v150, v151\n\t"
    "v_mfma_f32_16x16x32_bf16 v[132:135], a[188:191], a[76:79], v[132:135]\n\t"
    "ds_read_b64_tr_b16 a[216:217], v234 offset:57344\n\t"
    "v_exp_f32 v153, v153\n\t"
    "v_mfma_f32_16x16x32_bf16 v[136:139], a[160:163], a[80:83], v[180:183]\n\t"
    "v_exp_f32 v154, v154\n\t"
    "v_cvt_pk_bf16_f32 v212, v152, v153\n\t"
    "v_mfma_f32_16x16x32_bf16 v[136:139], a[164:167], a[84:87], v[136:139]\n\t"
    "ds_read_b64_tr_b16 a[218:219], v234 offset:61440\n\t"
    "v_exp_f32 v155, v155\n\t"
    "v_mfma_f32_16x16x32_bf16 v[136:139], a[168:171], a[88:91], v[136:139]\n\t"
    "v_exp_f32 v156, v156\n\t"
    "v_cvt_pk_bf16_f32 v213, v154, v155\n\t"
    "v_mfma_f32_16x16x32_bf16 v[136:139], a[172:175], a[92:95], v[136:139]\n\t"
    "ds_read_b64_tr_b16 a[220:221], v235 offset:57344\n\t"
    "v_exp_f32 v157, v157\n\t"
    "v_mfma_f32_16x16x32_bf16 v[140:143], a[176:179], a[80:83], v[180:183]\n\t"
    "v_exp_f32 v158, v158\n\t"
    "v_cvt_pk_bf16_f32 v214, v156, v157\n\t"
    "v_mfma_f32_16x16x32_bf16 v[140:143], a[180:183], a[84:87], v[140:143]\n\t"
    "ds_read_b64_tr_b16 a[222:223], v235 offset:61440\n\t"
    "v_exp_f32 v159, v159\n\t"
    "v_mfma_f32_16x16x32_bf16 v[140:143], a[184:187], a[88:91], v[140:143]\n\t"
    "v_fma_f32 v160, v160, s84, v192\n\t"
    "v_exp_f32 v160, v160\n\t"
    "v_cvt_pk_bf16_f32 v215, v158, v159\n\t"
    "v_mfma_f32_16x16x32_bf16 v[140:143], a[188:191], a[92:95], v[140:143]\n\t"
    "v_add_u32 v244, s63, v228\n\t"
    "ds_read_b64_tr_b16 a[224:225], v244 offset:32768\n\t"
    "v_fma_f32 v161, v161, s84, v192\n\t"
    "v_exp_f32 v161, v161\n\t"
    "v_mfma_f32_16x16x32_bf16 v[0:3], a[192:195], v[200:203], v[0:3]\n\t"
    "v_fma_f32 v162, v162, s84, v192\n\t"
    "v_exp_f32 v162, v162\n\t"
    "v_cvt_pk_bf16_f32 v160, v160, v161\n\t"
    "v_mfma_f32_16x16x32_bf16 v[32:35], a[192:195], v[204:207], v[32:35]\n\t"
    "ds_read_b64_tr_b16 a[226:227], v244 offset:36864\n\t"
    "v_fma_f32 v163, v163, s84, v192\n\t"
    "v_exp_f32 v163, v163\n\t"
    "v_mfma_f32_16x16x32_bf16 v[4:7], a[196:199], v[200:203], v[4:7]\n\t"
    "v_fma_f32 v164, v164, s84, v192\n\t"
    "v_exp_f32 v164, v164\n\t"
    "v_cvt_pk_bf16_f32 v161, v162, v163\n\t"
    "v_mfma_f32_16x16x32_bf16 v[36:39], a[196:199], v[204:207], v[36:39]\n\t"
    "v_add_u32 v245, s63, v229\n\t"
    "ds_read_b64_tr_b16 a[228:229], v245 offset:32768\n\t"
    "v_fma_f32 v165, v165, s84, v192\n\t"
    "v_exp_f32 v165, v165\n\t"
    "v_mfma_f32_16x16x32_bf16 v[8:11], a[200:203], v[200:203], v[8:11]\n\t"
    "v_fma_f32 v166, v166, s84, v192\n\t"
    "v_exp_f32 v166, v166\n\t"
    "v_cvt_pk_bf16_f32 v162, v164, v165\n\t"
    "v_mfma_f32_16x16x32_bf16 v[40:43], a[200:203], v[204:207], v[40:43]\n\t"
    "ds_read_b64_tr_b16 a[230:231], v245 offset:36864\n\t"
    "v_fma_f32 v167, v167, s84, v192\n\t"
    "v_exp_f32 v167, v167\n\t"
    "v_mfma_f32_16x16x32_bf16 v[12:15], a[204:207], v[200:203], v[12:15]\n\t"
    "v_fma_f32 v168, v168, s84, v193\n\t"
    "v_exp_f32 v168, v168\n\t"
    "v_cvt_pk_bf16_f32 v163, v166, v167\n\t"
    "v_mfma_f32_16x16x32_bf16 v[44:47], a[204:207], v[204:207], v[44:47]\n\t"
    "v_add_u32 v246, s63, v230\n\t"
    "ds_read_b64_tr_b16 a[232:233], v246 offset:32768\n\t"
    "v_fma_f32 v169, v169, s84, v193\n\t"
    "v_exp_f32 v169, v169\n\t"
    "s_waitcnt lgkmcnt(2)\n\t"
    "v_mfma_f32_16x16x32_bf16 v[16:19], a[208:211], v[200:203], v[16:19]\n\t"
    "v_fma_f32 v170, v170, s84, v193\n\t"
    "v_exp_f32 v170, v170\n\t"
    "v_cvt_pk_bf16_f32 v168, v168, v169\n\t"
    "v_mfma_f32_16x16x32_bf16 v[48:51], a[208:211], v[204:207], v[48:51]\n\t"
    "ds_read_b64_tr_b16 a[234:235], v246 offset:36864\n\t"
    "v_fma_f32 v171, v171, s84, v193\n\t"
    "v_exp_f32 v171, v171\n\t"
    "v_mfma_f32_16x16x32_bf16 v[20:23], a[212:215], v[200:203], v[20:23]\n\t"
    "v_fma_f32 v172, v172, s84, v193\n\t"
    "v_exp_f32 v172, v172\n\t"
    "v_cvt_pk_bf16_f32 v169, v170, v171\n\t"
    "v_mfma_f32_16x16x32_bf16 v[52:55], a[212:215], v[204:207], v[52:55]\n\t"
    "v_add_u32 v247, s63, v231\n\t"
    "ds_read_b64_tr_b16 a[236:237], v247 offset:32768\n\t"
    "v_fma_f32 v173, v173, s84, v193\n\t"
    "v_exp_f32 v173, v173\n\t"
    "v_mfma_f32_16x16x32_bf16 v[24:27], a[216:219], v[200:203], v[24:27]\n\t"
    "v_fma_f32 v174, v174, s84, v193\n\t"
    "v_exp_f32 v174, v174\n\t"
    "v_mfma_f32_16x16x32_bf16 v[56:59], a[216:219], v[204:207], v[56:59]\n\t"
    "ds_read_b64_tr_b16 a[238:239], v247 offset:36864\n\t"
    "v_fma_f32 v175, v175, s84, v193\n\t"
    "v_exp_f32 v175, v175\n\t"
    "v_mfma_f32_16x16x32_bf16 v[28:31], a[220:223], v[200:203], v[28:31]\n\t"
    "v_cvt_pk_bf16_f32 v170, v172, v173\n\t"
    "v_cvt_pk_bf16_f32 v171, v174, v175\n\t"
    "v_mfma_f32_16x16x32_bf16 v[60:63], a[220:223], v[204:207], v[60:63]\n\t"
    "v_mfma_f32_16x16x32_bf16 a[240:243], v[196:199], v[200:203], a[240:243]\n\t"
    "v_exp_f32 v128, v128\n\t"
    "v_mfma_f32_16x16x32_bf16 a[244:247], v[196:199], v[204:207], a[244:247]\n\t"
    "v_mfma_f32_16x16x32_bf16 a[0:3], a[224:227], v[160:163], a[0:3]\n\t"
    "v_exp_f32 v129, v129\n\t"
    "v_mfma_f32_16x16x32_bf16 a[32:35], a[224:227], v[168:171], a[32:35]\n\t"
    "v_add_u32 v244, s63, v232\n\t"
    "ds_read_b64_tr_b16 a[224:225], v244 offset:32768\n\t"
    "v_cvt_pk_bf16_f32 v200, v128, v129\n\t"
    "s_waitcnt lgkmcnt(1)\n\t"
    "v_mfma_f32_16x16x32_bf16 a[4:7], a[228:231], v[160:163], a[4:7]\n\t"
    "v_exp_f32 v130, v130\n\t"
    "v_mfma_f32_16x16x32_bf16 a[36:39], a[228:231], v[168:171], a[36:39]\n\t"
    "ds_read_b64_tr_b16 a[226:227], v244 offset:36864\n\t"
    "v_mfma_f32_16x16x32_bf16 a[8:11], a[232:235], v[160:163], a[8:11]\n\t"
    "v_exp_f32 v131, v131\n\t"
    "v_mfma_f32_16x16x32_bf16 a[40:43], a[232:235], v[168:171], a[40:43]\n\t"
    "v_add_u32 v245, s63, v233\n\t"
    "ds_read_b64_tr_b16 a[228:229], v245 offset:32768\n\t"
    "v_cvt_pk_bf16_f32 v201, v130, v131\n\t"
    "v_mfma_f32_16x16x32_bf16 a[12:15], a[236:239], v[160:163], a[12:15]\n\t"
    "v_exp_f32 v132, v132\n\t"
    "v_mfma_f32_16x16x32_bf16 a[44:47], a[236:239], v[168:171], a[44:47]\n\t"
    "ds_read_b64_tr_b16 a[230:231], v245 offset:36864\n\t"
    "v_mfma_f32_16x16x32_bf16 v[216:219], v[196:199], v[160:163], v[216:219]\n\t"
    "v_exp_f32 v133, v133\n\t"
    "v_mfma_f32_16x16x32_bf16 v[220:223], v[196:199], v[168:171], v[220:223]\n\t"
    "v_add_u32 v246, s63, v234\n\t"
    "ds_read_b64_tr_b16 a[232:233], v246 offset:32768\n\t"
    "v_cvt_pk_bf16_f32 v202, v132, v133\n\t"
    "s_waitcnt vmcnt(8)\n\t"
    "s_barrier\n\t"
    "v_mfma_f32_16x16x32_bf16 v[144:147], a[160:163], a[96:99], v[184:187]\n\t"
    "v_exp_f32 v134, v134\n\t"
    "v_mfma_f32_16x16x32_bf16 v[144:147], a[164:167], a[100:103], v[144:147]\n\t"
    "ds_read_b64_tr_b16 a[234:235], v246 offset:36864\n\t"
    "s_add_u32 m0, s80, 90112\n\t"
    "s_mov_b32 s81, 90112\n\t"
    "v_mfma_f32_16x16x32_bf16 v[144:147], a[168:171], a[104:107], v[144:147]\n\t"
    "v_exp_f32 v135, v135\n\t"
    "v_mfma_f32_16x16x32_bf16 v[144:147], a[172:175], a[108:111], v[144:147]\n\t"
    "v_add_u32 v247, s63, v235\n\t"
    "ds_read_b64_tr_b16 a[236:237], v247 offset:32768\n\t"
    "v_cvt_pk_bf16_f32 v203, v134, v135\n\t"
    "v_mfma_f32_16x16x32_bf16 v[148:151], a[176:179], a[96:99], v[184:187]\n\t"
    "v_exp_f32 v136, v136\n\t"
    "v_mfma_f32_16x16x32_bf16 v[148:151], a[180:183], a[100:103], v[148:151]\n\t"
    "ds_read_b64_tr_b16 a[238:239], v247 offset:36864\n\t"
    "buffer_load_dwordx4 v238, s[68:71], s81 offen nt lds\n\t"
    "v_mfma_f32_16x16x32_bf16 v[148:151], a[184:187], a[104:107], v[148:151]\n\t"
    "v_exp_f32 v137, v137\n\t"
    "v_mfma_f32_16x16x32_bf16 v[148:151], a[188:191], a[108:111], v[148:151]\n\t"
    "v_cvt_pk_bf16_f32 v204, v136, v137\n\t"
    "v_mfma_f32_16x16x32_bf16 v[152:155], a[160:163], a[112:115], v[188:191]\n\t"
    "v_exp_f32 v138, v138\n\t"
    "v_mfma_f32_16x16x32_bf16 v[152:155], a[164:167], a[116:119], v[152:155]\n\t"
    "ds_read_b128 a[160:163], v224 offset:8192\n\t"
    "v_mfma_f32_16x16x32_bf16 v[152:155], a[168:171], a[120:123], v[152:155]\n\t"
    "v_exp_f32 v139, v139\n\t"
    "v_mfma_f32_16x16x32_bf16 v[152:155], a[172:175], a[124:127], v[152:155]\n\t"
    "ds_read_b128 a[164:167], v225 offset:8192\n\t"
    "v_cvt_pk_bf16_f32 v205, v138, v139\n\t"
    "v_mfma_f32_16x16x32_bf16 v[156:159], a[176:179], a[112:115], v[188:191]\n\t"
    "v_exp_f32 v140, v140\n\t"
    "v_mfma_f32_16x16x32_bf16 v[156:159], a[180:183], a[116:119], v[156:159]\n\t"
    "ds_read_b128 a[168:171], v226 offset:8192\n\t"
    "buffer_load_dwordx4 v239, s[68:71], s81 offen offset:1024 nt lds\n\t"
    "v_mfma_f32_16x16x32_bf16 v[156:159], a[184:187], a[120:123], v[156:159]\n\t"
    "v_exp_f32 v141, v141\n\t"
    "v_mfma_f32_16x16x32_bf16 v[156:159], a[188:191], a[124:127], v[156:159]\n\t"
    "ds_read_b128 a[172:175], v227 offset:8192\n\t"
    "v_cvt_pk_bf16_f32 v206, v140, v141\n\t"
    "s_waitcnt lgkmcnt(2)\n\t"
    "v_mfma_f32_16x16x32_bf16 a[16:19], a[224:227], v[160:163], a[16:19]\n\t"
    "v_exp_f32 v142, v142\n\t"
    "v_mfma_f32_16x16x32_bf16 a[48:51], a[224:227], v[168:171], a[48:51]\n\t"
    "ds_read_b128 a[176:179], v224 offset:12288\n\t"
    "v_mfma_f32_16x16x32_bf16 a[20:23], a[228:231], v[160:163], a[20:23]\n\t"
    "v_exp_f32 v143, v143\n\t"
    "v_mfma_f32_16x16x32_bf16 a[52:55], a[228:231], v[168:171], a[52:55]\n\t"
    "ds_read_b128 a[180:183], v225 offset:12288\n\t"
    "v_cvt_pk_bf16_f32 v207, v142, v143\n\t"
    "s_add_u32 m0, s80, 32768\n\t"
    "v_mfma_f32_16x16x32_bf16 a[24:27], a[232:235], v[160:163], a[24:27]\n\t"
    "v_mfma_f32_16x16x32_bf16 a[56:59], a[232:235], v[168:171], a[56:59]\n\t"
    "ds_read_b128 a[184:187], v226 offset:12288\n\t"
    "s_mov_b32 s81, 98304\n\t"
    "v_mfma_f32_16x16x32_bf16 a[28:31], a[236:239], v[160:163], a[28:31]\n\t"
    "v_mfma_f32_16x16x32_bf16 a[60:63], a[236:239], v[168:171], a[60:63]\n\t"
    "ds_read_b128 a[188:191], v227 offset:12288\n\t"
    "buffer_load_dwordx4 v236, s[64:67], s81 offen nt lds\n\t"
    "v_mfma_f32_16x16x32_bf16 v[64:67], a[192:195], v[208:211], v[64:67]\n\t"
    "v_mfma_f32_16x16x32_bf16 v[96:99], a[192:195], v[212:215], v[96:99]\n\t"
    "ds_read_b64_tr_b16 a[192:193], v228 offset:0\n\t"
    "v_mfma_f32_16x16x32_bf16 v[68:71], a[196:199], v[208:211], v[68:71]\n\t"
    "v_mfma_f32_16x16x32_bf16 v[100:103], a[196:199], v[212:215], v[100:103]\n\t"
    "ds_read_b64_tr_b16 a[194:195], v228 offset:4096\n\t"
    "v_mfma_f32_16x16x32_bf16 v[72:75], a[200:203], v[208:211], v[72:75]\n\t"
    "v_mfma_f32_16x16x32_bf16 v[104:107], a[200:203], v[212:215], v[104:107]\n\t"
    "ds_read_b64_tr_b16 a[196:197], v229 offset:0\n\t"
    "v_mfma_f32_16x16x32_bf16 v[76:79], a[204:207], v[208:211], v[76:79]\n\t"
    "v_mfma_f32_16x16x32_bf16 v[108:111], a[204:207], v[212:215], v[108:111]\n\t"
    "ds_read_b64_tr_b16 a[198:199], v229 offset:4096\n\t"
    "buffer_load_dwordx4 v237, s[64:67], s81 offen offset:1024 nt lds\n\t"
    "v_mfma_f32_16x16x32_bf16 v[80:83], a[208:211], v[208:211], v[80:83]\n\t"
    "v_mfma_f32_16x16x32_bf16 v[112:115], a[208:211], v[212:215], v[112:115]\n\t"
    "ds_read_b64_tr_b16 a[200:201], v230 offset:0\n\t"
    "v_mfma_f32_16x16x32_bf16 v[84:87], a[212:215], v[208:211], v[84:87]\n\t"
    "v_mfma_f32_16x16x32_bf16 v[116:119], a[212:215], v[212:215], v[116:119]\n\t"
    "ds_read_b64_tr_b16 a[202:203], v230 offset:4096\n\t"
    "v_mfma_f32_16x16x32_bf16 v[88:91], a[216:219], v[208:211], v[88:91]\n\t"
    "v_mfma_f32_16x16x32_bf16 v[120:123], a[216:219], v[212:215], v[120:123]\n\t"
    "ds_read_b64_tr_b16 a[204:205], v231 offset:0\n\t"
    "v_mfma_f32_16x16x32_bf16 v[92:95], a[220:223], v[208:211], v[92:95]\n\t"
    "v_mfma_f32_16x16x32_bf16 v[124:127], a[220:223], v[212:215], v[124:127]\n\t"
    "ds_read_b64_tr_b16 a[206:207], v231 offset:4096\n\t"
    "v_mfma_f32_16x16x32_bf16 a[248:251], v[196:199], v[208:211], a[248:251]\n\t"
    "v_exp_f32 v144, v144\n\t"
    "v_mfma_f32_16x16x32_bf16 a[252:255], v[196:199], v[212:215], a[252:255]\n\t"
    "ds_read_b64_tr_b16 a[208:209], v232 offset:0\n\t"
    "v_mfma_f32_16x16x32_bf16 v[128:131], a[160:163], a[64:67], v[176:179]\n\t"
    "v_exp_f32 v145, v145\n\t"
    "v_mfma_f32_16x16x32_bf16 v[128:131], a[164:167], a[68:71], v[128:131]\n\t"
    "ds_read_b64_tr_b16 a[210:211], v232 offset:4096\n\t"
    "v_cvt_pk_bf16_f32 v208, v144, v145\n\t"
    "s_waitcnt lgkmcnt(2)\n\t"
    "v_mfma_f32_16x16x32_bf16 v[128:131], a[168:171], a[72:75], v[128:131]\n\t"
    "v_exp_f32 v146, v146\n\t"
    "v_mfma_f32_16x16x32_bf16 v[128:131], a[172:175], a[76:79], v[128:131]\n\t"
    "ds_read_b64_tr_b16 a[212:213], v233 offset:0\n\t"
    "v_mfma_f32_16x16x32_bf16 v[132:135], a[176:179], a[64:67], v[176:179]\n\t"
    "v_exp_f32 v147, v147\n\t"
    "v_mfma_f32_16x16x32_bf16 v[132:135], a[180:183], a[68:71], v[132:135]\n\t"
    "ds_read_b64_tr_b16 a[214:215], v233 offset:4096\n\t"
    "v_cvt_pk_bf16_f32 v209, v146, v147\n\t"
    "v_mfma_f32_16x16x32_bf16 v[132:135], a[184:187], a[72:75], v[132:135]\n\t"
    "v_exp_f32 v148, v148\n\t"
    "v_mfma_f32_16x16x32_bf16 v[132:135], a[188:191], a[76:79], v[132:135]\n\t"
    "ds_read_b64_tr_b16 a[216:217], v234 offset:0\n\t"
    "v_mfma_f32_16x16x32_bf16 v[136:139], a[160:163], a[80:83], v[180:183]\n\t"
    "v_exp_f32 v149, v149\n\t"
    "v_mfma_f32_16x16x32_bf16 v[136:139], a[164:167], a[84:87], v[136:139]\n\t"
    "ds_read_b64_tr_b16 a[218:219], v234 offset:4096\n\t"
    "v_cvt_pk_bf16_f32 v210, v148, v149\n\t"
    "v_mfma_f32_16x16x32_bf16 v[136:139], a[168:171], a[88:91], v[136:139]\n\t"
    "v_exp_f32 v150, v150\n\t"
    "v_mfma_f32_16x16x32_bf16 v[136:139], a[172:175], a[92:95], v[136:139]\n\t"
    "ds_read_b64_tr_b16 a[220:221], v235 offset:0\n\t"
    "v_mfma_f32_16x16x32_bf16 v[140:143], a[176:179], a[80:83], v[180:183]\n\t"
    "v_exp_f32 v151, v151\n\t"
    "v_mfma_f32_16x16x32_bf16 v[140:143], a[180:183], a[84:87], v[140:143]\n\t"
    "ds_read_b64_tr_b16 a[222:223], v235 offset:4096\n\t"
    "v_cvt_pk_bf16_f32 v211, v150, v151\n\t"
    "v_mfma_f32_16x16x32_bf16 v[140:143], a[184:187], a[88:91], v[140:143]\n\t"
    "v_exp_f32 v152, v152\n\t"
    "v_mfma_f32_16x16x32_bf16 v[140:143], a[188:191], a[92:95], v[140:143]\n\t"
    "v_mfma_f32_16x16x32_bf16 v[0:3], a[192:195], v[200:203], v[0:3]\n\t"
    "v_exp_f32 v153, v153\n\t"
    "v_mfma_f32_16x16x32_bf16 v[32:35], a[192:195], v[204:207], v[32:35]\n\t"
    "v_cvt_pk_bf16_f32 v212, v152, v153\n\t"
    "v_mfma_f32_16x16x32_bf16 v[4:7], a[196:199], v[200:203], v[4:7]\n\t"
    "v_exp_f32 v154, v154\n\t"
    "v_mfma_f32_16x16x32_bf16 v[36:39], a[196:199], v[204:207], v[36:39]\n\t"
    "v_mfma_f32_16x16x32_bf16 v[8:11], a[200:203], v[200:203], v[8:11]\n\t"
    "v_exp_f32 v155, v155\n\t"
    "v_mfma_f32_16x16x32_bf16 v[40:43], a[200:203], v[204:207], v[40:43]\n\t"
    "v_cvt_pk_bf16_f32 v213, v154, v155\n\t"
    "v_mfma_f32_16x16x32_bf16 v[12:15], a[204:207], v[200:203], v[12:15]\n\t"
    "v_exp_f32 v156, v156\n\t"
    "v_mfma_f32_16x16x32_bf16 v[44:47], a[204:207], v[204:207], v[44:47]\n\t"
    "s_waitcnt lgkmcnt(0)\n\t"
    "v_mfma_f32_16x16x32_bf16 v[16:19], a[208:211], v[200:203], v[16:19]\n\t"
    "v_exp_f32 v157, v157\n\t"
    "v_mfma_f32_16x16x32_bf16 v[48:51], a[208:211], v[204:207], v[48:51]\n\t"
    "v_cvt_pk_bf16_f32 v214, v156, v157\n\t"
    "v_mfma_f32_16x16x32_bf16 v[20:23], a[212:215], v[200:203], v[20:23]\n\t"
    "v_exp_f32 v158, v158\n\t"
    "v_mfma_f32_16x16x32_bf16 v[52:55], a[212:215], v[204:207], v[52:55]\n\t"
    "v_mfma_f32_16x16x32_bf16 v[24:27], a[216:219], v[200:203], v[24:27]\n\t"
    "v_exp_f32 v159, v159\n\t"
    "v_mfma_f32_16x16x32_bf16 v[56:59], a[216:219], v[204:207], v[56:59]\n\t"
    "v_cvt_pk_bf16_f32 v215, v158, v159\n\t"
    "v_mfma_f32_16x16x32_bf16 v[28:31], a[220:223], v[200:203], v[28:31]\n\t"
    "v_mfma_f32_16x16x32_bf16 v[60:63], a[220:223], v[204:207], v[60:63]\n\t"
    "v_mfma_f32_16x16x32_bf16 a[240:243], v[196:199], v[200:203], a[240:243]\n\t"
    "v_exp_f32 v128, v128\n\t"
    "v_mfma_f32_16x16x32_bf16 a[244:247], v[196:199], v[204:207], a[244:247]\n\t"
    "s_waitcnt vmcnt(8)\n\t"
    "s_barrier\n\t"
    "v_mfma_f32_16x16x32_bf16 v[144:147], a[160:163], a[96:99], v[184:187]\n\t"
    "v_exp_f32 v129, v129\n\t"
    "v_mfma_f32_16x16x32_bf16 v[144:147], a[164:167], a[100:103], v[144:147]\n\t"
    "v_cvt_pk_bf16_f32 v200, v128, v129\n\t"
    "s_add_u32 m0, s80, 98304\n\t"
    "v_mfma_f32_16x16x32_bf16 v[144:147], a[168:171], a[104:107], v[144:147]\n\t"
    "v_exp_f32 v130, v130\n\t"
    "v_mfma_f32_16x16x32_bf16 v[144:147], a[172:175], a[108:111], v[144:147]\n\t"
    "s_mov_b32 s81, 98304\n\t"
    "v_mfma_f32_16x16x32_bf16 v[148:151], a[176:179], a[96:99], v[184:187]\n\t"
    "v_exp_f32 v131, v131\n\t"
    "v_mfma_f32_16x16x32_bf16 v[148:151], a[180:183], a[100:103], v[148:151]\n\t"
    "v_cvt_pk_bf16_f32 v201, v130, v131\n\t"
    "v_mfma_f32_16x16x32_bf16 v[148:151], a[184:187], a[104:107], v[148:151]\n\t"
    "v_exp_f32 v132, v132\n\t"
    "v_mfma_f32_16x16x32_bf16 v[148:151], a[188:191], a[108:111], v[148:151]\n\t"
    "buffer_load_dwordx4 v238, s[68:71], s81 offen nt lds\n\t"
    "v_mfma_f32_16x16x32_bf16 v[152:155], a[160:163], a[112:115], v[188:191]\n\t"
    "v_exp_f32 v133, v133\n\t"
    "v_mfma_f32_16x16x32_bf16 v[152:155], a[164:167], a[116:119], v[152:155]\n\t"
    "ds_read_b128 a[160:163], v224 offset:16384\n\t"
    "v_cvt_pk_bf16_f32 v202, v132, v133\n\t"
    "v_mfma_f32_16x16x32_bf16 v[152:155], a[168:171], a[120:123], v[152:155]\n\t"
    "v_exp_f32 v134, v134\n\t"
    "v_mfma_f32_16x16x32_bf16 v[152:155], a[172:175], a[124:127], v[152:155]\n\t"
    "ds_read_b128 a[164:167], v225 offset:16384\n\t"
    "v_mfma_f32_16x16x32_bf16 v[156:159], a[176:179], a[112:115], v[188:191]\n\t"
    "v_exp_f32 v135, v135\n\t"
    "v_mfma_f32_16x16x32_bf16 v[156:159], a[180:183], a[116:119], v[156:159]\n\t"
    "ds_read_b128 a[168:171], v226 offset:16384\n\t"
    "v_cvt_pk_bf16_f32 v203, v134, v135\n\t"
    "v_mfma_f32_16x16x32_bf16 v[156:159], a[184:187], a[120:123], v[156:159]\n\t"
    "v_exp_f32 v136, v136\n\t"
    "v_mfma_f32_16x16x32_bf16 v[156:159], a[188:191], a[124:127], v[156:159]\n\t"
    "ds_read_b128 a[172:175], v227 offset:16384\n\t"
    "buffer_load_dwordx4 v239, s[68:71], s81 offen offset:1024 nt lds\n\t"
    "v_mfma_f32_16x16x32_bf16 v[64:67], a[192:195], v[208:211], v[64:67]\n\t"
    "v_exp_f32 v137, v137\n\t"
    "v_mfma_f32_16x16x32_bf16 v[96:99], a[192:195], v[212:215], v[96:99]\n\t"
    "ds_read_b128 a[176:179], v224 offset:20480\n\t"
    "v_cvt_pk_bf16_f32 v204, v136, v137\n\t"
    "v_mfma_f32_16x16x32_bf16 v[68:71], a[196:199], v[208:211], v[68:71]\n\t"
    "v_exp_f32 v138, v138\n\t"
    "v_mfma_f32_16x16x32_bf16 v[100:103], a[196:199], v[212:215], v[100:103]\n\t"
    "ds_read_b128 a[180:183], v225 offset:20480\n\t"
    "v_mfma_f32_16x16x32_bf16 v[72:75], a[200:203], v[208:211], v[72:75]\n\t"
    "v_exp_f32 v139, v139\n\t"
    "v_mfma_f32_16x16x32_bf16 v[104:107], a[200:203], v[212:215], v[104:107]\n\t"
    "ds_read_b128 a[184:187], v226 offset:20480\n\t"
    "v_cvt_pk_bf16_f32 v205, v138, v139\n\t"
    "s_add_u32 m0, s80, 40960\n\t"
    "v_mfma_f32_16x16x32_bf16 v[76:79], a[204:207], v[208:211], v[76:79]\n\t"
    "v_exp_f32 v140, v140\n\t"
    "v_mfma_f32_16x16x32_bf16 v[108:111], a[204:207], v[212:215], v[108:111]\n\t"
    "ds_read_b128 a[188:191], v227 offset:20480\n\t"
    "s_mov_b32 s81, 106496\n\t"
    "v_mfma_f32_16x16x32_bf16 v[80:83], a[208:211], v[208:211], v[80:83]\n\t"
    "v_exp_f32 v141, v141\n\t"
    "v_mfma_f32_16x16x32_bf16 v[112:115], a[208:211], v[212:215], v[112:115]\n\t"
    "ds_read_b64_tr_b16 a[192:193], v228 offset:8192\n\t"
    "v_cvt_pk_bf16_f32 v206, v140, v141\n\t"
    "v_mfma_f32_16x16x32_bf16 v[84:87], a[212:215], v[208:211], v[84:87]\n\t"
    "v_exp_f32 v142, v142\n\t"
    "v_mfma_f32_16x16x32_bf16 v[116:119], a[212:215], v[212:215], v[116:119]\n\t"
    "ds_read_b64_tr_b16 a[194:195], v228 offset:12288\n\t"
    "buffer_load_dwordx4 v236, s[64:67], s81 offen nt lds\n\t"
    "v_mfma_f32_16x16x32_bf16 v[88:91], a[216:219], v[208:211], v[88:91]\n\t"
    "v_exp_f32 v143, v143\n\t"
    "v_mfma_f32_16x16x32_bf16 v[120:123], a[216:219], v[212:215], v[120:123]\n\t"
    "ds_read_b64_tr_b16 a[196:197], v229 offset:8192\n\t"
    "v_cvt_pk_bf16_f32 v207, v142, v143\n\t"
    "v_mfma_f32_16x16x32_bf16 v[92:95], a[220:223], v[208:211], v[92:95]\n\t"
    "v_mfma_f32_16x16x32_bf16 v[124:127], a[220:223], v[212:215], v[124:127]\n\t"
    "ds_read_b64_tr_b16 a[198:199], v229 offset:12288\n\t"
    "v_mfma_f32_16x16x32_bf16 a[248:251], v[196:199], v[208:211], a[248:251]\n\t"
    "v_exp_f32 v144, v144\n\t"
    "v_mfma_f32_16x16x32_bf16 a[252:255], v[196:199], v[212:215], a[252:255]\n\t"
    "ds_read_b64_tr_b16 a[200:201], v230 offset:8192\n\t"
    "s_waitcnt lgkmcnt(2)\n\t"
    "v_mfma_f32_16x16x32_bf16 v[128:131], a[160:163], a[64:67], v[176:179]\n\t"
    "v_exp_f32 v145, v145\n\t"
    "v_mfma_f32_16x16x32_bf16 v[128:131], a[164:167], a[68:71], v[128:131]\n\t"
    "ds_read_b64_tr_b16 a[202:203], v230 offset:12288\n\t"
    "v_cvt_pk_bf16_f32 v208, v144, v145\n\t"
    "v_mfma_f32_16x16x32_bf16 v[128:131], a[168:171], a[72:75], v[128:131]\n\t"
    "v_exp_f32 v146, v146\n\t"
    "v_mfma_f32_16x16x32_bf16 v[128:131], a[172:175], a[76:79], v[128:131]\n\t"
    "ds_read_b64_tr_b16 a[204:205], v231 offset:8192\n\t"
    "buffer_load_dwordx4 v237, s[64:67], s81 offen offset:1024 nt lds\n\t"
    "v_mfma_f32_16x16x32_bf16 v[132:135], a[176:179], a[64:67], v[176:179]\n\t"
    "v_exp_f32 v147, v147\n\t"
    "v_mfma_f32_16x16x32_bf16 v[132:135], a[180:183], a[68:71], v[132:135]\n\t"
    "ds_read_b64_tr_b16 a[206:207], v231 offset:12288\n\t"
    "v_cvt_pk_bf16_f32 v209, v146, v147\n\t"
    "v_mfma_f32_16x16x32_bf16 v[132:135], a[184:187], a[72:75], v[132:135]\n\t"
    "v_exp_f32 v148, v148\n\t"
    "v_mfma_f32_16x16x32_bf16 v[132:135], a[188:191], a[76:79], v[132:135]\n\t"
    "ds_read_b64_tr_b16 a[208:209], v232 offset:8192\n\t"
    "v_mfma_f32_16x16x32_bf16 v[136:139], a[160:163], a[80:83], v[180:183]\n\t"
    "v_exp_f32 v149, v149\n\t"
    "v_mfma_f32_16x16x32_bf16 v[136:139], a[164:167], a[84:87], v[136:139]\n\t"
    "ds_read_b64_tr_b16 a[210:211], v232 offset:12288\n\t"
    "v_cvt_pk_bf16_f32 v210, v148, v149\n\t"
    "v_mfma_f32_16x16x32_bf16 v[136:139], a[168:171], a[88:91], v[136:139]\n\t"
    "v_exp_f32 v150, v150\n\t"
    "v_mfma_f32_16x16x32_bf16 v[136:139], a[172:175], a[92:95], v[136:139]\n\t"
    "ds_read_b64_tr_b16 a[212:213], v233 offset:8192\n\t"
    "v_mfma_f32_16x16x32_bf16 v[140:143], a[176:179], a[80:83], v[180:183]\n\t"
    "v_exp_f32 v151, v151\n\t"
    "v_mfma_f32_16x16x32_bf16 v[140:143], a[180:183], a[84:87], v[140:143]\n\t"
    "ds_read_b64_tr_b16 a[214:215], v233 offset:12288\n\t"
    "v_cvt_pk_bf16_f32 v211, v150, v151\n\t"
    "v_mfma_f32_16x16x32_bf16 v[140:143], a[184:187], a[88:91], v[140:143]\n\t"
    "v_exp_f32 v152, v152\n\t"
    "v_mfma_f32_16x16x32_bf16 v[140:143], a[188:191], a[92:95], v[140:143]\n\t"
    "ds_read_b64_tr_b16 a[216:217], v234 offset:8192\n\t"
    "v_mfma_f32_16x16x32_bf16 v[0:3], a[192:195], v[200:203], v[0:3]\n\t"
    "v_exp_f32 v153, v153\n\t"
    "v_mfma_f32_16x16x32_bf16 v[32:35], a[192:195], v[204:207], v[32:35]\n\t"
    "ds_read_b64_tr_b16 a[218:219], v234 offset:12288\n\t"
    "v_cvt_pk_bf16_f32 v212, v152, v153\n\t"
    "s_waitcnt lgkmcnt(2)\n\t"
    "v_mfma_f32_16x16x32_bf16 v[4:7], a[196:199], v[200:203], v[4:7]\n\t"
    "v_exp_f32 v154, v154\n\t"
    "v_mfma_f32_16x16x32_bf16 v[36:39], a[196:199], v[204:207], v[36:39]\n\t"
    "ds_read_b64_tr_b16 a[220:221], v235 offset:8192\n\t"
    "v_mfma_f32_16x16x32_bf16 v[8:11], a[200:203], v[200:203], v[8:11]\n\t"
    "v_exp_f32 v155, v155\n\t"
    "v_mfma_f32_16x16x32_bf16 v[40:43], a[200:203], v[204:207], v[40:43]\n\t"
    "ds_read_b64_tr_b16 a[222:223], v235 offset:12288\n\t"
    "v_cvt_pk_bf16_f32 v213, v154, v155\n\t"
    "v_mfma_f32_16x16x32_bf16 v[12:15], a[204:207], v[200:203], v[12:15]\n\t"
    "v_exp_f32 v156, v156\n\t"
    "v_mfma_f32_16x16x32_bf16 v[44:47], a[204:207], v[204:207], v[44:47]\n\t"
    "v_mfma_f32_16x16x32_bf16 v[16:19], a[208:211], v[200:203], v[16:19]\n\t"
    "v_exp_f32 v157, v157\n\t"
    "v_mfma_f32_16x16x32_bf16 v[48:51], a[208:211], v[204:207], v[48:51]\n\t"
    "v_cvt_pk_bf16_f32 v214, v156, v157\n\t"
    "v_mfma_f32_16x16x32_bf16 v[20:23], a[212:215], v[200:203], v[20:23]\n\t"
    "v_exp_f32 v158, v158\n\t"
    "v_mfma_f32_16x16x32_bf16 v[52:55], a[212:215], v[204:207], v[52:55]\n\t"
    "s_waitcnt lgkmcnt(0)\n\t"
    "v_mfma_f32_16x16x32_bf16 v[24:27], a[216:219], v[200:203], v[24:27]\n\t"
    "v_exp_f32 v159, v159\n\t"
    "v_mfma_f32_16x16x32_bf16 v[56:59], a[216:219], v[204:207], v[56:59]\n\t"
    "v_cvt_pk_bf16_f32 v215, v158, v159\n\t"
    "v_mfma_f32_16x16x32_bf16 v[28:31], a[220:223], v[200:203], v[28:31]\n\t"
    "v_mfma_f32_16x16x32_bf16 v[60:63], a[220:223], v[204:207], v[60:63]\n\t"
    "v_mfma_f32_16x16x32_bf16 a[240:243], v[196:199], v[200:203], a[240:243]\n\t"
    "v_exp_f32 v128, v128\n\t"
    "v_mfma_f32_16x16x32_bf16 a[244:247], v[196:199], v[204:207], a[244:247]\n\t"
    "s_waitcnt vmcnt(8)\n\t"
    "s_barrier\n\t"
    "v_mfma_f32_16x16x32_bf16 v[144:147], a[160:163], a[96:99], v[184:187]\n\t"
    "v_exp_f32 v129, v129\n\t"
    "v_mfma_f32_16x16x32_bf16 v[144:147], a[164:167], a[100:103], v[144:147]\n\t"
    "v_cvt_pk_bf16_f32 v200, v128, v129\n\t"
    "s_add_u32 m0, s80, 106496\n\t"
    "v_mfma_f32_16x16x32_bf16 v[144:147], a[168:171], a[104:107], v[144:147]\n\t"
    "v_exp_f32 v130, v130\n\t"
    "v_mfma_f32_16x16x32_bf16 v[144:147], a[172:175], a[108:111], v[144:147]\n\t"
    "s_mov_b32 s81, 106496\n\t"
    "v_mfma_f32_16x16x32_bf16 v[148:151], a[176:179], a[96:99], v[184:187]\n\t"
    "v_exp_f32 v131, v131\n\t"
    "v_mfma_f32_16x16x32_bf16 v[148:151], a[180:183], a[100:103], v[148:151]\n\t"
    "v_cvt_pk_bf16_f32 v201, v130, v131\n\t"
    "v_mfma_f32_16x16x32_bf16 v[148:151], a[184:187], a[104:107], v[148:151]\n\t"
    "v_exp_f32 v132, v132\n\t"
    "v_mfma_f32_16x16x32_bf16 v[148:151], a[188:191], a[108:111], v[148:151]\n\t"
    "buffer_load_dwordx4 v238, s[68:71], s81 offen nt lds\n\t"
    "v_mfma_f32_16x16x32_bf16 v[152:155], a[160:163], a[112:115], v[188:191]\n\t"
    "v_exp_f32 v133, v133\n\t"
    "v_mfma_f32_16x16x32_bf16 v[152:155], a[164:167], a[116:119], v[152:155]\n\t"
    "ds_read_b128 a[160:163], v224 offset:24576\n\t"
    "v_cvt_pk_bf16_f32 v202, v132, v133\n\t"
    "v_mfma_f32_16x16x32_bf16 v[152:155], a[168:171], a[120:123], v[152:155]\n\t"
    "v_exp_f32 v134, v134\n\t"
    "v_mfma_f32_16x16x32_bf16 v[152:155], a[172:175], a[124:127], v[152:155]\n\t"
    "ds_read_b128 a[164:167], v225 offset:24576\n\t"
    "v_mfma_f32_16x16x32_bf16 v[156:159], a[176:179], a[112:115], v[188:191]\n\t"
    "v_exp_f32 v135, v135\n\t"
    "v_mfma_f32_16x16x32_bf16 v[156:159], a[180:183], a[116:119], v[156:159]\n\t"
    "ds_read_b128 a[168:171], v226 offset:24576\n\t"
    "v_cvt_pk_bf16_f32 v203, v134, v135\n\t"
    "v_mfma_f32_16x16x32_bf16 v[156:159], a[184:187], a[120:123], v[156:159]\n\t"
    "v_exp_f32 v136, v136\n\t"
    "v_mfma_f32_16x16x32_bf16 v[156:159], a[188:191], a[124:127], v[156:159]\n\t"
    "ds_read_b128 a[172:175], v227 offset:24576\n\t"
    "buffer_load_dwordx4 v239, s[68:71], s81 offen offset:1024 nt lds\n\t"
    "v_mfma_f32_16x16x32_bf16 v[64:67], a[192:195], v[208:211], v[64:67]\n\t"
    "v_exp_f32 v137, v137\n\t"
    "v_mfma_f32_16x16x32_bf16 v[96:99], a[192:195], v[212:215], v[96:99]\n\t"
    "ds_read_b128 a[176:179], v224 offset:28672\n\t"
    "v_cvt_pk_bf16_f32 v204, v136, v137\n\t"
    "v_mfma_f32_16x16x32_bf16 v[68:71], a[196:199], v[208:211], v[68:71]\n\t"
    "v_exp_f32 v138, v138\n\t"
    "v_mfma_f32_16x16x32_bf16 v[100:103], a[196:199], v[212:215], v[100:103]\n\t"
    "ds_read_b128 a[180:183], v225 offset:28672\n\t"
    "v_mfma_f32_16x16x32_bf16 v[72:75], a[200:203], v[208:211], v[72:75]\n\t"
    "v_exp_f32 v139, v139\n\t"
    "v_mfma_f32_16x16x32_bf16 v[104:107], a[200:203], v[212:215], v[104:107]\n\t"
    "ds_read_b128 a[184:187], v226 offset:28672\n\t"
    "v_cvt_pk_bf16_f32 v205, v138, v139\n\t"
    "s_add_u32 m0, s80, 49152\n\t"
    "v_mfma_f32_16x16x32_bf16 v[76:79], a[204:207], v[208:211], v[76:79]\n\t"
    "v_exp_f32 v140, v140\n\t"
    "v_mfma_f32_16x16x32_bf16 v[108:111], a[204:207], v[212:215], v[108:111]\n\t"
    "ds_read_b128 a[188:191], v227 offset:28672\n\t"
    "s_mov_b32 s81, 114688\n\t"
    "v_mfma_f32_16x16x32_bf16 v[80:83], a[208:211], v[208:211], v[80:83]\n\t"
    "v_exp_f32 v141, v141\n\t"
    "v_mfma_f32_16x16x32_bf16 v[112:115], a[208:211], v[212:215], v[112:115]\n\t"
    "ds_read_b64_tr_b16 a[192:193], v228 offset:16384\n\t"
    "v_cvt_pk_bf16_f32 v206, v140, v141\n\t"
    "v_mfma_f32_16x16x32_bf16 v[84:87], a[212:215], v[208:211], v[84:87]\n\t"
    "v_exp_f32 v142, v142\n\t"
    "v_mfma_f32_16x16x32_bf16 v[116:119], a[212:215], v[212:215], v[116:119]\n\t"
    "ds_read_b64_tr_b16 a[194:195], v228 offset:20480\n\t"
    "buffer_load_dwordx4 v236, s[64:67], s81 offen nt lds\n\t"
    "v_mfma_f32_16x16x32_bf16 v[88:91], a[216:219], v[208:211], v[88:91]\n\t"
    "v_exp_f32 v143, v143\n\t"
    "v_mfma_f32_16x16x32_bf16 v[120:123], a[216:219], v[212:215], v[120:123]\n\t"
    "ds_read_b64_tr_b16 a[196:197], v229 offset:16384\n\t"
    "v_cvt_pk_bf16_f32 v207, v142, v143\n\t"
    "v_mfma_f32_16x16x32_bf16 v[92:95], a[220:223], v[208:211], v[92:95]\n\t"
    "v_mfma_f32_16x16x32_bf16 v[124:127], a[220:223], v[212:215], v[124:127]\n\t"
    "ds_read_b64_tr_b16 a[198:199], v229 offset:20480\n\t"
    "v_mfma_f32_16x16x32_bf16 a[248:251], v[196:199], v[208:211], a[248:251]\n\t"
    "v_exp_f32 v144, v144\n\t"
    "v_mfma_f32_16x16x32_bf16 a[252:255], v[196:199], v[212:215], a[252:255]\n\t"
    "ds_read_b64_tr_b16 a[200:201], v230 offset:16384\n\t"
    "s_waitcnt lgkmcnt(2)\n\t"
    "v_mfma_f32_16x16x32_bf16 v[128:131], a[160:163], a[64:67], v[176:179]\n\t"
    "v_exp_f32 v145, v145\n\t"
    "v_mfma_f32_16x16x32_bf16 v[128:131], a[164:167], a[68:71], v[128:131]\n\t"
    "ds_read_b64_tr_b16 a[202:203], v230 offset:20480\n\t"
    "v_cvt_pk_bf16_f32 v208, v144, v145\n\t"
    "v_mfma_f32_16x16x32_bf16 v[128:131], a[168:171], a[72:75], v[128:131]\n\t"
    "v_exp_f32 v146, v146\n\t"
    "v_mfma_f32_16x16x32_bf16 v[128:131], a[172:175], a[76:79], v[128:131]\n\t"
    "ds_read_b64_tr_b16 a[204:205], v231 offset:16384\n\t"
    "buffer_load_dwordx4 v237, s[64:67], s81 offen offset:1024 nt lds\n\t"
    "v_mfma_f32_16x16x32_bf16 v[132:135], a[176:179], a[64:67], v[176:179]\n\t"
    "v_exp_f32 v147, v147\n\t"
    "v_mfma_f32_16x16x32_bf16 v[132:135], a[180:183], a[68:71], v[132:135]\n\t"
    "ds_read_b64_tr_b16 a[206:207], v231 offset:20480\n\t"
    "v_cvt_pk_bf16_f32 v209, v146, v147\n\t"
    "v_mfma_f32_16x16x32_bf16 v[132:135], a[184:187], a[72:75], v[132:135]\n\t"
    "v_exp_f32 v148, v148\n\t"
    "v_mfma_f32_16x16x32_bf16 v[132:135], a[188:191], a[76:79], v[132:135]\n\t"
    "ds_read_b64_tr_b16 a[208:209], v232 offset:16384\n\t"
    "v_mfma_f32_16x16x32_bf16 v[136:139], a[160:163], a[80:83], v[180:183]\n\t"
    "v_exp_f32 v149, v149\n\t"
    "v_mfma_f32_16x16x32_bf16 v[136:139], a[164:167], a[84:87], v[136:139]\n\t"
    "ds_read_b64_tr_b16 a[210:211], v232 offset:20480\n\t"
    "v_cvt_pk_bf16_f32 v210, v148, v149\n\t"
    "v_mfma_f32_16x16x32_bf16 v[136:139], a[168:171], a[88:91], v[136:139]\n\t"
    "v_exp_f32 v150, v150\n\t"
    "v_mfma_f32_16x16x32_bf16 v[136:139], a[172:175], a[92:95], v[136:139]\n\t"
    "ds_read_b64_tr_b16 a[212:213], v233 offset:16384\n\t"
    "v_mfma_f32_16x16x32_bf16 v[140:143], a[176:179], a[80:83], v[180:183]\n\t"
    "v_exp_f32 v151, v151\n\t"
    "v_mfma_f32_16x16x32_bf16 v[140:143], a[180:183], a[84:87], v[140:143]\n\t"
    "ds_read_b64_tr_b16 a[214:215], v233 offset:20480\n\t"
    "v_cvt_pk_bf16_f32 v211, v150, v151\n\t"
    "v_mfma_f32_16x16x32_bf16 v[140:143], a[184:187], a[88:91], v[140:143]\n\t"
    "v_exp_f32 v152, v152\n\t"
    "v_mfma_f32_16x16x32_bf16 v[140:143], a[188:191], a[92:95], v[140:143]\n\t"
    "ds_read_b64_tr_b16 a[216:217], v234 offset:16384\n\t"
    "v_mfma_f32_16x16x32_bf16 v[0:3], a[192:195], v[200:203], v[0:3]\n\t"
    "v_exp_f32 v153, v153\n\t"
    "v_mfma_f32_16x16x32_bf16 v[32:35], a[192:195], v[204:207], v[32:35]\n\t"
    "ds_read_b64_tr_b16 a[218:219], v234 offset:20480\n\t"
    "v_cvt_pk_bf16_f32 v212, v152, v153\n\t"
    "s_waitcnt lgkmcnt(2)\n\t"
    "v_mfma_f32_16x16x32_bf16 v[4:7], a[196:199], v[200:203], v[4:7]\n\t"
    "v_exp_f32 v154, v154\n\t"
    "v_mfma_f32_16x16x32_bf16 v[36:39], a[196:199], v[204:207], v[36:39]\n\t"
    "ds_read_b64_tr_b16 a[220:221], v235 offset:16384\n\t"
    "v_mfma_f32_16x16x32_bf16 v[8:11], a[200:203], v[200:203], v[8:11]\n\t"
    "v_exp_f32 v155, v155\n\t"
    "v_mfma_f32_16x16x32_bf16 v[40:43], a[200:203], v[204:207], v[40:43]\n\t"
    "ds_read_b64_tr_b16 a[222:223], v235 offset:20480\n\t"
    "v_cvt_pk_bf16_f32 v213, v154, v155\n\t"
    "v_mfma_f32_16x16x32_bf16 v[12:15], a[204:207], v[200:203], v[12:15]\n\t"
    "v_exp_f32 v156, v156\n\t"
    "v_mfma_f32_16x16x32_bf16 v[44:47], a[204:207], v[204:207], v[44:47]\n\t"
    "v_add_u32 v240, s63, v224\n\t"
    "ds_read_b128 a[224:227], v240 offset:0\n\t"
    "v_mfma_f32_16x16x32_bf16 v[16:19], a[208:211], v[200:203], v[16:19]\n\t"
    "v_exp_f32 v157, v157\n\t"
    "v_mfma_f32_16x16x32_bf16 v[48:51], a[208:211], v[204:207], v[48:51]\n\t"
    "v_add_u32 v241, s63, v225\n\t"
    "ds_read_b128 a[228:231], v241 offset:0\n\t"
    "v_cvt_pk_bf16_f32 v214, v156, v157\n\t"
    "v_mfma_f32_16x16x32_bf16 v[20:23], a[212:215], v[200:203], v[20:23]\n\t"
    "v_exp_f32 v158, v158\n\t"
    "v_mfma_f32_16x16x32_bf16 v[52:55], a[212:215], v[204:207], v[52:55]\n\t"
    "v_add_u32 v242, s63, v226\n\t"
    "ds_read_b128 a[232:235], v242 offset:0\n\t"
    "s_waitcnt lgkmcnt(2)\n\t"
    "v_mfma_f32_16x16x32_bf16 v[24:27], a[216:219], v[200:203], v[24:27]\n\t"
    "v_exp_f32 v159, v159\n\t"
    "v_mfma_f32_16x16x32_bf16 v[56:59], a[216:219], v[204:207], v[56:59]\n\t"
    "v_add_u32 v243, s63, v227\n\t"
    "ds_read_b128 a[236:239], v243 offset:0\n\t"
    "v_cvt_pk_bf16_f32 v215, v158, v159\n\t"
    "v_mfma_f32_16x16x32_bf16 v[28:31], a[220:223], v[200:203], v[28:31]\n\t"
    "v_mfma_f32_16x16x32_bf16 v[60:63], a[220:223], v[204:207], v[60:63]\n\t"
    "v_mfma_f32_16x16x32_bf16 a[240:243], v[196:199], v[200:203], a[240:243]\n\t"
    "v_exp_f32 v128, v128\n\t"
    "v_mfma_f32_16x16x32_bf16 a[244:247], v[196:199], v[204:207], a[244:247]\n\t"
    "v_mfma_f32_16x16x32_bf16 v[160:163], a[224:227], a[128:131], 0\n\t"
    "v_exp_f32 v129, v129\n\t"
    "s_waitcnt lgkmcnt(0)\n\t"
    "v_mfma_f32_16x16x32_bf16 v[160:163], a[228:231], a[132:135], v[160:163]\n\t"
    "v_cvt_pk_bf16_f32 v200, v128, v129\n\t"
    "v_mfma_f32_16x16x32_bf16 v[160:163], a[232:235], a[136:139], v[160:163]\n\t"
    "v_exp_f32 v130, v130\n\t"
    "v_mfma_f32_16x16x32_bf16 v[160:163], a[236:239], a[140:143], v[160:163]\n\t"
    "v_mfma_f32_16x16x32_bf16 v[168:171], a[224:227], a[144:147], 0\n\t"
    "v_exp_f32 v131, v131\n\t"
    "v_mfma_f32_16x16x32_bf16 v[168:171], a[228:231], a[148:151], v[168:171]\n\t"
    "ds_read_b128 a[224:227], v240 offset:4096\n\t"
    "v_cvt_pk_bf16_f32 v201, v130, v131\n\t"
    "v_mfma_f32_16x16x32_bf16 v[168:171], a[232:235], a[152:155], v[168:171]\n\t"
    "v_exp_f32 v132, v132\n\t"
    "v_mfma_f32_16x16x32_bf16 v[168:171], a[236:239], a[156:159], v[168:171]\n\t"
    "ds_read_b128 a[228:231], v241 offset:4096\n\t"
    "s_waitcnt vmcnt(8)\n\t"
    "s_barrier\n\t"
    "v_mfma_f32_16x16x32_bf16 v[144:147], a[160:163], a[96:99], v[184:187]\n\t"
    "v_exp_f32 v133, v133\n\t"
    "v_mfma_f32_16x16x32_bf16 v[144:147], a[164:167], a[100:103], v[144:147]\n\t"
    "ds_read_b128 a[232:235], v242 offset:4096\n\t"
    "v_cvt_pk_bf16_f32 v202, v132, v133\n\t"
    "s_add_u32 m0, s80, 114688\n\t"
    "v_mfma_f32_16x16x32_bf16 v[144:147], a[168:171], a[104:107], v[144:147]\n\t"
    "v_exp_f32 v134, v134\n\t"
    "v_mfma_f32_16x16x32_bf16 v[144:147], a[172:175], a[108:111], v[144:147]\n\t"
    "ds_read_b128 a[236:239], v243 offset:4096\n\t"
    "s_mov_b32 s81, 114688\n\t"
    "v_mfma_f32_16x16x32_bf16 v[148:151], a[176:179], a[96:99], v[184:187]\n\t"
    "v_exp_f32 v135, v135\n\t"
    "v_mfma_f32_16x16x32_bf16 v[148:151], a[180:183], a[100:103], v[148:151]\n\t"
    "v_cvt_pk_bf16_f32 v203, v134, v135\n\t"
    "v_mfma_f32_16x16x32_bf16 v[148:151], a[184:187], a[104:107], v[148:151]\n\t"
    "v_exp_f32 v136, v136\n\t"
    "v_mfma_f32_16x16x32_bf16 v[148:151], a[188:191], a[108:111], v[148:151]\n\t"
    "buffer_load_dwordx4 v238, s[68:71], s81 offen nt lds\n\t"
    "v_mfma_f32_16x16x32_bf16 v[152:155], a[160:163], a[112:115], v[188:191]\n\t"
    "v_exp_f32 v137, v137\n\t"
    "v_mfma_f32_16x16x32_bf16 v[152:155], a[164:167], a[116:119], v[152:155]\n\t"
    "ds_read_b128 a[160:163], v224 offset:32768\n\t"
    "v_cvt_pk_bf16_f32 v204, v136, v137\n\t"
    "v_mfma_f32_16x16x32_bf16 v[152:155], a[168:171], a[120:123], v[152:155]\n\t"
    "v_exp_f32 v138, v138\n\t"
    "v_mfma_f32_16x16x32_bf16 v[152:155], a[172:175], a[124:127], v[152:155]\n\t"
    "ds_read_b128 a[164:167], v225 offset:32768\n\t"
    "v_mfma_f32_16x16x32_bf16 v[156:159], a[176:179], a[112:115], v[188:191]\n\t"
    "v_exp_f32 v139, v139\n\t"
    "v_mfma_f32_16x16x32_bf16 v[156:159], a[180:183], a[116:119], v[156:159]\n\t"
    "ds_read_b128 a[168:171], v226 offset:32768\n\t"
    "v_cvt_pk_bf16_f32 v205, v138, v139\n\t"
    "v_mfma_f32_16x16x32_bf16 v[156:159], a[184:187], a[120:123], v[156:159]\n\t"
    "v_exp_f32 v140, v140\n\t"
    "v_mfma_f32_16x16x32_bf16 v[156:159], a[188:191], a[124:127], v[156:159]\n\t"
    "ds_read_b128 a[172:175], v227 offset:32768\n\t"
    "buffer_load_dwordx4 v239, s[68:71], s81 offen offset:1024 nt lds\n\t"
    "s_waitcnt lgkmcnt(2)\n\t"
    "v_mfma_f32_16x16x32_bf16 v[164:167], a[224:227], a[128:131], 0\n\t"
    "v_exp_f32 v141, v141\n\t"
    "v_mfma_f32_16x16x32_bf16 v[164:167], a[228:231], a[132:135], v[164:167]\n\t"
    "ds_read_b128 a[176:179], v224 offset:36864\n\t"
    "v_cvt_pk_bf16_f32 v206, v140, v141\n\t"
    "v_mfma_f32_16x16x32_bf16 v[164:167], a[232:235], a[136:139], v[164:167]\n\t"
    "v_exp_f32 v142, v142\n\t"
    "v_mfma_f32_16x16x32_bf16 v[164:167], a[236:239], a[140:143], v[164:167]\n\t"
    "ds_read_b128 a[180:183], v225 offset:36864\n\t"
    "v_mfma_f32_16x16x32_bf16 v[172:175], a[224:227], a[144:147], 0\n\t"
    "v_exp_f32 v143, v143\n\t"
    "v_mfma_f32_16x16x32_bf16 v[172:175], a[228:231], a[148:151], v[172:175]\n\t"
    "ds_read_b128 a[184:187], v226 offset:36864\n\t"
    "v_cvt_pk_bf16_f32 v207, v142, v143\n\t"
    "s_add_u32 m0, s80, 57344\n\t"
    "v_mfma_f32_16x16x32_bf16 v[172:175], a[232:235], a[152:155], v[172:175]\n\t"
    "v_mfma_f32_16x16x32_bf16 v[172:175], a[236:239], a[156:159], v[172:175]\n\t"
    "ds_read_b128 a[188:191], v227 offset:36864\n\t"
    "s_mov_b32 s81, 122880\n\t"
    "v_mfma_f32_16x16x32_bf16 v[64:67], a[192:195], v[208:211], v[64:67]\n\t"
    "v_mfma_f32_16x16x32_bf16 v[96:99], a[192:195], v[212:215], v[96:99]\n\t"
    "ds_read_b64_tr_b16 a[192:193], v228 offset:24576\n\t"
    "buffer_load_dwordx4 v236, s[64:67], s81 offen nt lds\n\t"
    "v_mfma_f32_16x16x32_bf16 v[68:71], a[196:199], v[208:211], v[68:71]\n\t"
    "v_mfma_f32_16x16x32_bf16 v[100:103], a[196:199], v[212:215], v[100:103]\n\t"
    "ds_read_b64_tr_b16 a[194:195], v228 offset:28672\n\t"
    "v_mfma_f32_16x16x32_bf16 v[72:75], a[200:203], v[208:211], v[72:75]\n\t"
    "v_mfma_f32_16x16x32_bf16 v[104:107], a[200:203], v[212:215], v[104:107]\n\t"
    "ds_read_b64_tr_b16 a[196:197], v229 offset:24576\n\t"
    "v_mfma_f32_16x16x32_bf16 v[76:79], a[204:207], v[208:211], v[76:79]\n\t"
    "v_mfma_f32_16x16x32_bf16 v[108:111], a[204:207], v[212:215], v[108:111]\n\t"
    "ds_read_b64_tr_b16 a[198:199], v229 offset:28672\n\t"
    "v_mfma_f32_16x16x32_bf16 v[80:83], a[208:211], v[208:211], v[80:83]\n\t"
    "v_mfma_f32_16x16x32_bf16 v[112:115], a[208:211], v[212:215], v[112:115]\n\t"
    "ds_read_b64_tr_b16 a[200:201], v230 offset:24576\n\t"
    "buffer_load_dwordx4 v237, s[64:67], s81 offen offset:1024 nt lds\n\t"
    "v_mfma_f32_16x16x32_bf16 v[84:87], a[212:215], v[208:211], v[84:87]\n\t"
    "v_mfma_f32_16x16x32_bf16 v[116:119], a[212:215], v[212:215], v[116:119]\n\t"
    "ds_read_b64_tr_b16 a[202:203], v230 offset:28672\n\t"
    "v_mfma_f32_16x16x32_bf16 v[88:91], a[216:219], v[208:211], v[88:91]\n\t"
    "v_mfma_f32_16x16x32_bf16 v[120:123], a[216:219], v[212:215], v[120:123]\n\t"
    "ds_read_b64_tr_b16 a[204:205], v231 offset:24576\n\t"
    "v_mfma_f32_16x16x32_bf16 v[92:95], a[220:223], v[208:211], v[92:95]\n\t"
    "v_mfma_f32_16x16x32_bf16 v[124:127], a[220:223], v[212:215], v[124:127]\n\t"
    "ds_read_b64_tr_b16 a[206:207], v231 offset:28672\n\t"
    "v_mfma_f32_16x16x32_bf16 a[248:251], v[196:199], v[208:211], a[248:251]\n\t"
    "v_exp_f32 v144, v144\n\t"
    "v_mfma_f32_16x16x32_bf16 a[252:255], v[196:199], v[212:215], a[252:255]\n\t"
    "ds_read_b64_tr_b16 a[208:209], v232 offset:24576\n\t"
    "v_exp_f32 v145, v145\n\t"
    "v_mfma_f32_16x16x32_bf16 v[128:131], a[160:163], a[64:67], v[176:179]\n\t"
    "v_exp_f32 v146, v146\n\t"
    "v_cvt_pk_bf16_f32 v208, v144, v145\n\t"
    "v_mfma_f32_16x16x32_bf16 v[128:131], a[164:167], a[68:71], v[128:131]\n\t"
    "ds_read_b64_tr_b16 a[210:211], v232 offset:28672\n\t"
    "v_exp_f32 v147, v147\n\t"
    "s_waitcnt lgkmcnt(2)\n\t"
    "v_mfma_f32_16x16x32_bf16 v[128:131], a[168:171], a[72:75], v[128:131]\n\t"
    "v_exp_f32 v148, v148\n\t"
    "v_cvt_pk_bf16_f32 v209, v146, v147\n\t"
    "v_mfma_f32_16x16x32_bf16 v[128:131], a[172:175], a[76:79], v[128:131]\n\t"
    "ds_read_b64_tr_b16 a[212:213], v233 offset:24576\n\t"
    "v_exp_f32 v149, v149\n\t"
    "v_mfma_f32_16x16x32_bf16 v[132:135], a[176:179], a[64:67], v[176:179]\n\t"
    "v_exp_f32 v150, v150\n\t"
    "v_cvt_pk_bf16_f32 v210, v148, v149\n\t"
    "v_mfma_f32_16x16x32_bf16 v[132:135], a[180:183], a[68:71], v[132:135]\n\t"
    "ds_read_b64_tr_b16 a[214:215], v233 offset:28672\n\t"
    "v_exp_f32 v151, v151\n\t"
    "v_mfma_f32_16x16x32_bf16 v[132:135], a[184:187], a[72:75], v[132:135]\n\t"
    "v_exp_f32 v152, v152\n\t"
    "v_cvt_pk_bf16_f32 v211, v150, v151\n\t"
    "v_mfma_f32_16x16x32_bf16 v[132:135], a[188:191], a[76:79], v[132:135]\n\t"
    "ds_read_b64_tr_b16 a[216:217], v234 offset:24576\n\t"
    "v_exp_f32 v153, v153\n\t"
    "v_mfma_f32_16x16x32_bf16 v[136:139], a[160:163], a[80:83], v[180:183]\n\t"
    "v_exp_f32 v154, v154\n\t"
    "v_cvt_pk_bf16_f32 v212, v152, v153\n\t"
    "v_mfma_f32_16x16x32_bf16 v[136:139], a[164:167], a[84:87], v[136:139]\n\t"
    "ds_read_b64_tr_b16 a[218:219], v234 offset:28672\n\t"
    "v_exp_f32 v155, v155\n\t"
    "v_mfma_f32_16x16x32_bf16 v[136:139], a[168:171], a[88:91], v[136:139]\n\t"
    "v_exp_f32 v156, v156\n\t"
    "v_cvt_pk_bf16_f32 v213, v154, v155\n\t"
    "v_mfma_f32_16x16x32_bf16 v[136:139], a[172:175], a[92:95], v[136:139]\n\t"
    "ds_read_b64_tr_b16 a[220:221], v235 offset:24576\n\t"
    "v_exp_f32 v157, v157\n\t"
    "v_mfma_f32_16x16x32_bf16 v[140:143], a[176:179], a[80:83], v[180:183]\n\t"
    "v_exp_f32 v158, v158\n\t"
    "v_cvt_pk_bf16_f32 v214, v156, v157\n\t"
    "v_mfma_f32_16x16x32_bf16 v[140:143], a[180:183], a[84:87], v[140:143]\n\t"
    "ds_read_b64_tr_b16 a[222:223], v235 offset:28672\n\t"
    "v_exp_f32 v159, v159\n\t"
    "v_mfma_f32_16x16x32_bf16 v[140:143], a[184:187], a[88:91], v[140:143]\n\t"
    "v_fma_f32 v160, v160, s84, v192\n\t"
    "v_exp_f32 v160, v160\n\t"
    "v_cvt_pk_bf16_f32 v215, v158, v159\n\t"
    "v_mfma_f32_16x16x32_bf16 v[140:143], a[188:191], a[92:95], v[140:143]\n\t"
    "v_add_u32 v244, s63, v228\n\t"
    "ds_read_b64_tr_b16 a[224:225], v244 offset:0\n\t"
    "v_fma_f32 v161, v161, s84, v192\n\t"
    "v_exp_f32 v161, v161\n\t"
    "v_mfma_f32_16x16x32_bf16 v[0:3], a[192:195], v[200:203], v[0:3]\n\t"
    "v_fma_f32 v162, v162, s84, v192\n\t"
    "v_exp_f32 v162, v162\n\t"
    "v_cvt_pk_bf16_f32 v160, v160, v161\n\t"
    "v_mfma_f32_16x16x32_bf16 v[32:35], a[192:195], v[204:207], v[32:35]\n\t"
    "ds_read_b64_tr_b16 a[226:227], v244 offset:4096\n\t"
    "v_fma_f32 v163, v163, s84, v192\n\t"
    "v_exp_f32 v163, v163\n\t"
    "v_mfma_f32_16x16x32_bf16 v[4:7], a[196:199], v[200:203], v[4:7]\n\t"
    "v_fma_f32 v164, v164, s84, v192\n\t"
    "v_exp_f32 v164, v164\n\t"
    "v_cvt_pk_bf16_f32 v161, v162, v163\n\t"
    "v_mfma_f32_16x16x32_bf16 v[36:39], a[196:199], v[204:207], v[36:39]\n\t"
    "v_add_u32 v245, s63, v229\n\t"
    "ds_read_b64_tr_b16 a[228:229], v245 offset:0\n\t"
    "v_fma_f32 v165, v165, s84, v192\n\t"
    "v_exp_f32 v165, v165\n\t"
    "v_mfma_f32_16x16x32_bf16 v[8:11], a[200:203], v[200:203], v[8:11]\n\t"
    "v_fma_f32 v166, v166, s84, v192\n\t"
    "v_exp_f32 v166, v166\n\t"
    "v_cvt_pk_bf16_f32 v162, v164, v165\n\t"
    "v_mfma_f32_16x16x32_bf16 v[40:43], a[200:203], v[204:207], v[40:43]\n\t"
    "ds_read_b64_tr_b16 a[230:231], v245 offset:4096\n\t"
    "v_fma_f32 v167, v167, s84, v192\n\t"
    "v_exp_f32 v167, v167\n\t"
    "v_mfma_f32_16x16x32_bf16 v[12:15], a[204:207], v[200:203], v[12:15]\n\t"
    "v_fma_f32 v168, v168, s84, v193\n\t"
    "v_exp_f32 v168, v168\n\t"
    "v_cvt_pk_bf16_f32 v163, v166, v167\n\t"
    "v_mfma_f32_16x16x32_bf16 v[44:47], a[204:207], v[204:207], v[44:47]\n\t"
    "v_add_u32 v246, s63, v230\n\t"
    "ds_read_b64_tr_b16 a[232:233], v246 offset:0\n\t"
    "v_fma_f32 v169, v169, s84, v193\n\t"
    "v_exp_f32 v169, v169\n\t"
    "s_waitcnt lgkmcnt(2)\n\t"
    "v_mfma_f32_16x16x32_bf16 v[16:19], a[208:211], v[200:203], v[16:19]\n\t"
    "v_fma_f32 v170, v170, s84, v193\n\t"
    "v_exp_f32 v170, v170\n\t"
    "v_cvt_pk_bf16_f32 v168, v168, v169\n\t"
    "v_mfma_f32_16x16x32_bf16 v[48:51], a[208:211], v[204:207], v[48:51]\n\t"
    "ds_read_b64_tr_b16 a[234:235], v246 offset:4096\n\t"
    "v_fma_f32 v171, v171, s84, v193\n\t"
    "v_exp_f32 v171, v171\n\t"
    "v_mfma_f32_16x16x32_bf16 v[20:23], a[212:215], v[200:203], v[20:23]\n\t"
    "v_fma_f32 v172, v172, s84, v193\n\t"
    "v_exp_f32 v172, v172\n\t"
    "v_cvt_pk_bf16_f32 v169, v170, v171\n\t"
    "v_mfma_f32_16x16x32_bf16 v[52:55], a[212:215], v[204:207], v[52:55]\n\t"
    "v_add_u32 v247, s63, v231\n\t"
    "ds_read_b64_tr_b16 a[236:237], v247 offset:0\n\t"
    "v_fma_f32 v173, v173, s84, v193\n\t"
    "v_exp_f32 v173, v173\n\t"
    "v_mfma_f32_16x16x32_bf16 v[24:27], a[216:219], v[200:203], v[24:27]\n\t"
    "v_fma_f32 v174, v174, s84, v193\n\t"
    "v_exp_f32 v174, v174\n\t"
    "v_mfma_f32_16x16x32_bf16 v[56:59], a[216:219], v[204:207], v[56:59]\n\t"
    "ds_read_b64_tr_b16 a[238:239], v247 offset:4096\n\t"
    "v_fma_f32 v175, v175, s84, v193\n\t"
    "v_exp_f32 v175, v175\n\t"
    "v_mfma_f32_16x16x32_bf16 v[28:31], a[220:223], v[200:203], v[28:31]\n\t"
    "v_cvt_pk_bf16_f32 v170, v172, v173\n\t"
    "v_cvt_pk_bf16_f32 v171, v174, v175\n\t"
    "v_mfma_f32_16x16x32_bf16 v[60:63], a[220:223], v[204:207], v[60:63]\n\t"
    "v_mfma_f32_16x16x32_bf16 a[240:243], v[196:199], v[200:203], a[240:243]\n\t"
    "v_exp_f32 v128, v128\n\t"
    "v_mfma_f32_16x16x32_bf16 a[244:247], v[196:199], v[204:207], a[244:247]\n\t"
    "v_mfma_f32_16x16x32_bf16 a[0:3], a[224:227], v[160:163], a[0:3]\n\t"
    "v_exp_f32 v129, v129\n\t"
    "v_mfma_f32_16x16x32_bf16 a[32:35], a[224:227], v[168:171], a[32:35]\n\t"
    "v_add_u32 v244, s63, v232\n\t"
    "ds_read_b64_tr_b16 a[224:225], v244 offset:0\n\t"
    "v_cvt_pk_bf16_f32 v200, v128, v129\n\t"
    "s_waitcnt lgkmcnt(1)\n\t"
    "v_mfma_f32_16x16x32_bf16 a[4:7], a[228:231], v[160:163], a[4:7]\n\t"
    "v_exp_f32 v130, v130\n\t"
    "v_mfma_f32_16x16x32_bf16 a[36:39], a[228:231], v[168:171], a[36:39]\n\t"
    "ds_read_b64_tr_b16 a[226:227], v244 offset:4096\n\t"
    "v_mfma_f32_16x16x32_bf16 a[8:11], a[232:235], v[160:163], a[8:11]\n\t"
    "v_exp_f32 v131, v131\n\t"
    "v_mfma_f32_16x16x32_bf16 a[40:43], a[232:235], v[168:171], a[40:43]\n\t"
    "v_add_u32 v245, s63, v233\n\t"
    "ds_read_b64_tr_b16 a[228:229], v245 offset:0\n\t"
    "v_cvt_pk_bf16_f32 v201, v130, v131\n\t"
    "v_mfma_f32_16x16x32_bf16 a[12:15], a[236:239], v[160:163], a[12:15]\n\t"
    "v_exp_f32 v132, v132\n\t"
    "v_mfma_f32_16x16x32_bf16 a[44:47], a[236:239], v[168:171], a[44:47]\n\t"
    "ds_read_b64_tr_b16 a[230:231], v245 offset:4096\n\t"
    "v_mfma_f32_16x16x32_bf16 v[216:219], v[196:199], v[160:163], v[216:219]\n\t"
    "v_exp_f32 v133, v133\n\t"
    "v_mfma_f32_16x16x32_bf16 v[220:223], v[196:199], v[168:171], v[220:223]\n\t"
    "v_add_u32 v246, s63, v234\n\t"
    "ds_read_b64_tr_b16 a[232:233], v246 offset:0\n\t"
    "v_cvt_pk_bf16_f32 v202, v132, v133\n\t"
    "s_waitcnt vmcnt(8)\n\t"
    "s_barrier\n\t"
    "v_mfma_f32_16x16x32_bf16 v[144:147], a[160:163], a[96:99], v[184:187]\n\t"
    "v_exp_f32 v134, v134\n\t"
    "v_mfma_f32_16x16x32_bf16 v[144:147], a[164:167], a[100:103], v[144:147]\n\t"
    "ds_read_b64_tr_b16 a[234:235], v246 offset:4096\n\t"
    "s_add_u32 m0, s80, 122880\n\t"
    "s_mov_b32 s81, 122880\n\t"
    "v_mfma_f32_16x16x32_bf16 v[144:147], a[168:171], a[104:107], v[144:147]\n\t"
    "v_exp_f32 v135, v135\n\t"
    "v_mfma_f32_16x16x32_bf16 v[144:147], a[172:175], a[108:111], v[144:147]\n\t"
    "v_add_u32 v247, s63, v235\n\t"
    "ds_read_b64_tr_b16 a[236:237], v247 offset:0\n\t"
    "v_cvt_pk_bf16_f32 v203, v134, v135\n\t"
    "v_mfma_f32_16x16x32_bf16 v[148:151], a[176:179], a[96:99], v[184:187]\n\t"
    "v_exp_f32 v136, v136\n\t"
    "v_mfma_f32_16x16x32_bf16 v[148:151], a[180:183], a[100:103], v[148:151]\n\t"
    "ds_read_b64_tr_b16 a[238:239], v247 offset:4096\n\t"
    "buffer_load_dwordx4 v238, s[68:71], s81 offen nt lds\n\t"
    "v_mfma_f32_16x16x32_bf16 v[148:151], a[184:187], a[104:107], v[148:151]\n\t"
    "v_exp_f32 v137, v137\n\t"
    "v_mfma_f32_16x16x32_bf16 v[148:151], a[188:191], a[108:111], v[148:151]\n\t"
    "v_cvt_pk_bf16_f32 v204, v136, v137\n\t"
    "v_mfma_f32_16x16x32_bf16 v[152:155], a[160:163], a[112:115], v[188:191]\n\t"
    "v_exp_f32 v138, v138\n\t"
    "v_mfma_f32_16x16x32_bf16 v[152:155], a[164:167], a[116:119], v[152:155]\n\t"
    "ds_read_b128 a[160:163], v224 offset:40960\n\t"
    "v_mfma_f32_16x16x32_bf16 v[152:155], a[168:171], a[120:123], v[152:155]\n\t"
    "v_exp_f32 v139, v139\n\t"
    "v_mfma_f32_16x16x32_bf16 v[152:155], a[172:175], a[124:127], v[152:155]\n\t"
    "ds_read_b128 a[164:167], v225 offset:40960\n\t"
    "v_cvt_pk_bf16_f32 v205, v138, v139\n\t"
    "v_mfma_f32_16x16x32_bf16 v[156:159], a[176:179], a[112:115], v[188:191]\n\t"
    "v_exp_f32 v140, v140\n\t"
    "v_mfma_f32_16x16x32_bf16 v[156:159], a[180:183], a[116:119], v[156:159]\n\t"
    "ds_read_b128 a[168:171], v226 offset:40960\n\t"
    "buffer_load_dwordx4 v239, s[68:71], s81 offen offset:1024 nt lds\n\t"
    "v_mfma_f32_16x16x32_bf16 v[156:159], a[184:187], a[120:123], v[156:159]\n\t"
    "v_exp_f32 v141, v141\n\t"
    "v_mfma_f32_16x16x32_bf16 v[156:159], a[188:191], a[124:127], v[156:159]\n\t"
    "ds_read_b128 a[172:175], v227 offset:40960\n\t"
    "v_cvt_pk_bf16_f32 v206, v140, v141\n\t"
    "s_waitcnt lgkmcnt(2)\n\t"
    "v_mfma_f32_16x16x32_bf16 a[16:19], a[224:227], v[160:163], a[16:19]\n\t"
    "v_exp_f32 v142, v142\n\t"
    "v_mfma_f32_16x16x32_bf16 a[48:51], a[224:227], v[168:171], a[48:51]\n\t"
    "ds_read_b128 a[176:179], v224 offset:45056\n\t"
    "v_mfma_f32_16x16x32_bf16 a[20:23], a[228:231], v[160:163], a[20:23]\n\t"
    "v_exp_f32 v143, v143\n\t"
    "v_mfma_f32_16x16x32_bf16 a[52:55], a[228:231], v[168:171], a[52:55]\n\t"
    "ds_read_b128 a[180:183], v225 offset:45056\n\t"
    "v_cvt_pk_bf16_f32 v207, v142, v143\n\t"
    "v_mfma_f32_16x16x32_bf16 a[24:27], a[232:235], v[160:163], a[24:27]\n\t"
    "v_mfma_f32_16x16x32_bf16 a[56:59], a[232:235], v[168:171], a[56:59]\n\t"
    "ds_read_b128 a[184:187], v226 offset:45056\n\t"
    "v_mfma_f32_16x16x32_bf16 a[28:31], a[236:239], v[160:163], a[28:31]\n\t"
    "v_mfma_f32_16x16x32_bf16 a[60:63], a[236:239], v[168:171], a[60:63]\n\t"
    "ds_read_b128 a[188:191], v227 offset:45056\n\t"
    "v_mfma_f32_16x16x32_bf16 v[64:67], a[192:195], v[208:211], v[64:67]\n\t"
    "v_mfma_f32_16x16x32_bf16 v[96:99], a[192:195], v[212:215], v[96:99]\n\t"
    "ds_read_b64_tr_b16 a[192:193], v228 offset:32768\n\t"
    "v_mfma_f32_16x16x32_bf16 v[68:71], a[196:199], v[208:211], v[68:71]\n\t"
    "v_mfma_f32_16x16x32_bf16 v[100:103], a[196:199], v[212:215], v[100:103]\n\t"
    "ds_read_b64_tr_b16 a[194:195], v228 offset:36864\n\t"
    "v_mfma_f32_16x16x32_bf16 v[72:75], a[200:203], v[208:211], v[72:75]\n\t"
    "v_mfma_f32_16x16x32_bf16 v[104:107], a[200:203], v[212:215], v[104:107]\n\t"
    "ds_read_b64_tr_b16 a[196:197], v229 offset:32768\n\t"
    "v_mfma_f32_16x16x32_bf16 v[76:79], a[204:207], v[208:211], v[76:79]\n\t"
    "v_mfma_f32_16x16x32_bf16 v[108:111], a[204:207], v[212:215], v[108:111]\n\t"
    "ds_read_b64_tr_b16 a[198:199], v229 offset:36864\n\t"
    "v_mfma_f32_16x16x32_bf16 v[80:83], a[208:211], v[208:211], v[80:83]\n\t"
    "v_mfma_f32_16x16x32_bf16 v[112:115], a[208:211], v[212:215], v[112:115]\n\t"
    "ds_read_b64_tr_b16 a[200:201], v230 offset:32768\n\t"
    "v_mfma_f32_16x16x32_bf16 v[84:87], a[212:215], v[208:211], v[84:87]\n\t"
    "v_mfma_f32_16x16x32_bf16 v[116:119], a[212:215], v[212:215], v[116:119]\n\t"
    "ds_read_b64_tr_b16 a[202:203], v230 offset:36864\n\t"
    "v_mfma_f32_16x16x32_bf16 v[88:91], a[216:219], v[208:211], v[88:91]\n\t"
    "v_mfma_f32_16x16x32_bf16 v[120:123], a[216:219], v[212:215], v[120:123]\n\t"
    "ds_read_b64_tr_b16 a[204:205], v231 offset:32768\n\t"
    "v_mfma_f32_16x16x32_bf16 v[92:95], a[220:223], v[208:211], v[92:95]\n\t"
    "v_mfma_f32_16x16x32_bf16 v[124:127], a[220:223], v[212:215], v[124:127]\n\t"
    "ds_read_b64_tr_b16 a[206:207], v231 offset:36864\n\t"
    "v_mfma_f32_16x16x32_bf16 a[248:251], v[196:199], v[208:211], a[248:251]\n\t"
    "v_exp_f32 v144, v144\n\t"
    "v_mfma_f32_16x16x32_bf16 a[252:255], v[196:199], v[212:215], a[252:255]\n\t"
    "ds_read_b64_tr_b16 a[208:209], v232 offset:32768\n\t"
    "v_mfma_f32_16x16x32_bf16 v[128:131], a[160:163], a[64:67], v[176:179]\n\t"
    "v_exp_f32 v145, v145\n\t"
    "v_mfma_f32_16x16x32_bf16 v[128:131], a[164:167], a[68:71], v[128:131]\n\t"
    "ds_read_b64_tr_b16 a[210:211], v232 offset:36864\n\t"
    "v_cvt_pk_bf16_f32 v208, v144, v145\n\t"
    "s_waitcnt lgkmcnt(2)\n\t"
    "v_mfma_f32_16x16x32_bf16 v[128:131], a[168:171], a[72:75], v[128:131]\n\t"
    "v_exp_f32 v146, v146\n\t"
    "v_mfma_f32_16x16x32_bf16 v[128:131], a[172:175], a[76:79], v[128:131]\n\t"
    "ds_read_b64_tr_b16 a[212:213], v233 offset:32768\n\t"
    "v_mfma_f32_16x16x32_bf16 v[132:135], a[176:179], a[64:67], v[176:179]\n\t"
    "v_exp_f32 v147, v147\n\t"
    "v_mfma_f32_16x16x32_bf16 v[132:135], a[180:183], a[68:71], v[132:135]\n\t"
    "ds_read_b64_tr_b16 a[214:215], v233 offset:36864\n\t"
    "v_cvt_pk_bf16_f32 v209, v146, v147\n\t"
    "v_mfma_f32_16x16x32_bf16 v[132:135], a[184:187], a[72:75], v[132:135]\n\t"
    "v_exp_f32 v148, v148\n\t"
    "v_mfma_f32_16x16x32_bf16 v[132:135], a[188:191], a[76:79], v[132:135]\n\t"
    "ds_read_b64_tr_b16 a[216:217], v234 offset:32768\n\t"
    "v_mfma_f32_16x16x32_bf16 v[136:139], a[160:163], a[80:83], v[180:183]\n\t"
    "v_exp_f32 v149, v149\n\t"
    "v_mfma_f32_16x16x32_bf16 v[136:139], a[164:167], a[84:87], v[136:139]\n\t"
    "ds_read_b64_tr_b16 a[218:219], v234 offset:36864\n\t"
    "v_cvt_pk_bf16_f32 v210, v148, v149\n\t"
    "v_mfma_f32_16x16x32_bf16 v[136:139], a[168:171], a[88:91], v[136:139]\n\t"
    "v_exp_f32 v150, v150\n\t"
    "v_mfma_f32_16x16x32_bf16 v[136:139], a[172:175], a[92:95], v[136:139]\n\t"
    "ds_read_b64_tr_b16 a[220:221], v235 offset:32768\n\t"
    "v_mfma_f32_16x16x32_bf16 v[140:143], a[176:179], a[80:83], v[180:183]\n\t"
    "v_exp_f32 v151, v151\n\t"
    "v_mfma_f32_16x16x32_bf16 v[140:143], a[180:183], a[84:87], v[140:143]\n\t"
    "ds_read_b64_tr_b16 a[222:223], v235 offset:36864\n\t"
    "v_cvt_pk_bf16_f32 v211, v150, v151\n\t"
    "v_mfma_f32_16x16x32_bf16 v[140:143], a[184:187], a[88:91], v[140:143]\n\t"
    "v_exp_f32 v152, v152\n\t"
    "v_mfma_f32_16x16x32_bf16 v[140:143], a[188:191], a[92:95], v[140:143]\n\t"
    "v_mfma_f32_16x16x32_bf16 v[0:3], a[192:195], v[200:203], v[0:3]\n\t"
    "v_exp_f32 v153, v153\n\t"
    "v_mfma_f32_16x16x32_bf16 v[32:35], a[192:195], v[204:207], v[32:35]\n\t"
    "v_cvt_pk_bf16_f32 v212, v152, v153\n\t"
    "v_mfma_f32_16x16x32_bf16 v[4:7], a[196:199], v[200:203], v[4:7]\n\t"
    "v_exp_f32 v154, v154\n\t"
    "v_mfma_f32_16x16x32_bf16 v[36:39], a[196:199], v[204:207], v[36:39]\n\t"
    "v_mfma_f32_16x16x32_bf16 v[8:11], a[200:203], v[200:203], v[8:11]\n\t"
    "v_exp_f32 v155, v155\n\t"
    "v_mfma_f32_16x16x32_bf16 v[40:43], a[200:203], v[204:207], v[40:43]\n\t"
    "v_cvt_pk_bf16_f32 v213, v154, v155\n\t"
    "v_mfma_f32_16x16x32_bf16 v[12:15], a[204:207], v[200:203], v[12:15]\n\t"
    "v_exp_f32 v156, v156\n\t"
    "v_mfma_f32_16x16x32_bf16 v[44:47], a[204:207], v[204:207], v[44:47]\n\t"
    "s_waitcnt lgkmcnt(0)\n\t"
    "v_mfma_f32_16x16x32_bf16 v[16:19], a[208:211], v[200:203], v[16:19]\n\t"
    "v_exp_f32 v157, v157\n\t"
    "v_mfma_f32_16x16x32_bf16 v[48:51], a[208:211], v[204:207], v[48:51]\n\t"
    "v_cvt_pk_bf16_f32 v214, v156, v157\n\t"
    "v_mfma_f32_16x16x32_bf16 v[20:23], a[212:215], v[200:203], v[20:23]\n\t"
    "v_exp_f32 v158, v158\n\t"
    "v_mfma_f32_16x16x32_bf16 v[52:55], a[212:215], v[204:207], v[52:55]\n\t"
    "v_mfma_f32_16x16x32_bf16 v[24:27], a[216:219], v[200:203], v[24:27]\n\t"
    "v_exp_f32 v159, v159\n\t"
    "v_mfma_f32_16x16x32_bf16 v[56:59], a[216:219], v[204:207], v[56:59]\n\t"
    "v_cvt_pk_bf16_f32 v215, v158, v159\n\t"
    "v_mfma_f32_16x16x32_bf16 v[28:31], a[220:223], v[200:203], v[28:31]\n\t"
    "v_mfma_f32_16x16x32_bf16 v[60:63], a[220:223], v[204:207], v[60:63]\n\t"
    "v_mfma_f32_16x16x32_bf16 a[240:243], v[196:199], v[200:203], a[240:243]\n\t"
    "v_exp_f32 v128, v128\n\t"
    "v_mfma_f32_16x16x32_bf16 a[244:247], v[196:199], v[204:207], a[244:247]\n\t"
    "s_waitcnt vmcnt(2)\n\t"
    "s_barrier\n\t"
    "v_mfma_f32_16x16x32_bf16 v[144:147], a[160:163], a[96:99], v[184:187]\n\t"
    "v_exp_f32 v129, v129\n\t"
    "v_mfma_f32_16x16x32_bf16 v[144:147], a[164:167], a[100:103], v[144:147]\n\t"
    "v_cvt_pk_bf16_f32 v200, v128, v129\n\t"
    "v_mfma_f32_16x16x32_bf16 v[144:147], a[168:171], a[104:107], v[144:147]\n\t"
    "v_exp_f32 v130, v130\n\t"
    "v_mfma_f32_16x16x32_bf16 v[144:147], a[172:175], a[108:111], v[144:147]\n\t"
    "v_mfma_f32_16x16x32_bf16 v[148:151], a[176:179], a[96:99], v[184:187]\n\t"
    "v_exp_f32 v131, v131\n\t"
    "v_mfma_f32_16x16x32_bf16 v[148:151], a[180:183], a[100:103], v[148:151]\n\t"
    "v_cvt_pk_bf16_f32 v201, v130, v131\n\t"
    "v_mfma_f32_16x16x32_bf16 v[148:151], a[184:187], a[104:107], v[148:151]\n\t"
    "v_exp_f32 v132, v132\n\t"
    "v_mfma_f32_16x16x32_bf16 v[148:151], a[188:191], a[108:111], v[148:151]\n\t"
    "v_mfma_f32_16x16x32_bf16 v[152:155], a[160:163], a[112:115], v[188:191]\n\t"
    "v_exp_f32 v133, v133\n\t"
    "v_mfma_f32_16x16x32_bf16 v[152:155], a[164:167], a[116:119], v[152:155]\n\t"
    "ds_read_b128 a[160:163], v224 offset:49152\n\t"
    "v_cvt_pk_bf16_f32 v202, v132, v133\n\t"
    "v_mfma_f32_16x16x32_bf16 v[152:155], a[168:171], a[120:123], v[152:155]\n\t"
    "v_exp_f32 v134, v134\n\t"
    "v_mfma_f32_16x16x32_bf16 v[152:155], a[172:175], a[124:127], v[152:155]\n\t"
    "ds_read_b128 a[164:167], v225 offset:49152\n\t"
    "v_mfma_f32_16x16x32_bf16 v[156:159], a[176:179], a[112:115], v[188:191]\n\t"
    "v_exp_f32 v135, v135\n\t"
    "v_mfma_f32_16x16x32_bf16 v[156:159], a[180:183], a[116:119], v[156:159]\n\t"
    "ds_read_b128 a[168:171], v226 offset:49152\n\t"
    "v_cvt_pk_bf16_f32 v203, v134, v135\n\t"
    "v_mfma_f32_16x16x32_bf16 v[156:159], a[184:187], a[120:123], v[156:159]\n\t"
    "v_exp_f32 v136, v136\n\t"
    "v_mfma_f32_16x16x32_bf16 v[156:159], a[188:191], a[124:127], v[156:159]\n\t"
    "ds_read_b128 a[172:175], v227 offset:49152\n\t"
    "v_mfma_f32_16x16x32_bf16 v[64:67], a[192:195], v[208:211], v[64:67]\n\t"
    "v_exp_f32 v137, v137\n\t"
    "v_mfma_f32_16x16x32_bf16 v[96:99], a[192:195], v[212:215], v[96:99]\n\t"
    "ds_read_b128 a[176:179], v224 offset:53248\n\t"
    "v_cvt_pk_bf16_f32 v204, v136, v137\n\t"
    "v_mfma_f32_16x16x32_bf16 v[68:71], a[196:199], v[208:211], v[68:71]\n\t"
    "v_exp_f32 v138, v138\n\t"
    "v_mfma_f32_16x16x32_bf16 v[100:103], a[196:199], v[212:215], v[100:103]\n\t"
    "ds_read_b128 a[180:183], v225 offset:53248\n\t"
    "v_mfma_f32_16x16x32_bf16 v[72:75], a[200:203], v[208:211], v[72:75]\n\t"
    "v_exp_f32 v139, v139\n\t"
    "v_mfma_f32_16x16x32_bf16 v[104:107], a[200:203], v[212:215], v[104:107]\n\t"
    "ds_read_b128 a[184:187], v226 offset:53248\n\t"
    "v_cvt_pk_bf16_f32 v205, v138, v139\n\t"
    "v_mfma_f32_16x16x32_bf16 v[76:79], a[204:207], v[208:211], v[76:79]\n\t"
    "v_exp_f32 v140, v140\n\t"
    "v_mfma_f32_16x16x32_bf16 v[108:111], a[204:207], v[212:215], v[108:111]\n\t"
    "ds_read_b128 a[188:191], v227 offset:53248\n\t"
    "v_mfma_f32_16x16x32_bf16 v[80:83], a[208:211], v[208:211], v[80:83]\n\t"
    "v_exp_f32 v141, v141\n\t"
    "v_mfma_f32_16x16x32_bf16 v[112:115], a[208:211], v[212:215], v[112:115]\n\t"
    "ds_read_b64_tr_b16 a[192:193], v228 offset:40960\n\t"
    "v_cvt_pk_bf16_f32 v206, v140, v141\n\t"
    "v_mfma_f32_16x16x32_bf16 v[84:87], a[212:215], v[208:211], v[84:87]\n\t"
    "v_exp_f32 v142, v142\n\t"
    "v_mfma_f32_16x16x32_bf16 v[116:119], a[212:215], v[212:215], v[116:119]\n\t"
    "ds_read_b64_tr_b16 a[194:195], v228 offset:45056\n\t"
    "v_mfma_f32_16x16x32_bf16 v[88:91], a[216:219], v[208:211], v[88:91]\n\t"
    "v_exp_f32 v143, v143\n\t"
    "v_mfma_f32_16x16x32_bf16 v[120:123], a[216:219], v[212:215], v[120:123]\n\t"
    "ds_read_b64_tr_b16 a[196:197], v229 offset:40960\n\t"
    "v_cvt_pk_bf16_f32 v207, v142, v143\n\t"
    "v_mfma_f32_16x16x32_bf16 v[92:95], a[220:223], v[208:211], v[92:95]\n\t"
    "v_mfma_f32_16x16x32_bf16 v[124:127], a[220:223], v[212:215], v[124:127]\n\t"
    "ds_read_b64_tr_b16 a[198:199], v229 offset:45056\n\t"
    "v_mfma_f32_16x16x32_bf16 a[248:251], v[196:199], v[208:211], a[248:251]\n\t"
    "v_exp_f32 v144, v144\n\t"
    "v_mfma_f32_16x16x32_bf16 a[252:255], v[196:199], v[212:215], a[252:255]\n\t"
    "ds_read_b64_tr_b16 a[200:201], v230 offset:40960\n\t"
    "s_waitcnt lgkmcnt(2)\n\t"
    "v_mfma_f32_16x16x32_bf16 v[128:131], a[160:163], a[64:67], v[176:179]\n\t"
    "v_exp_f32 v145, v145\n\t"
    "v_mfma_f32_16x16x32_bf16 v[128:131], a[164:167], a[68:71], v[128:131]\n\t"
    "ds_read_b64_tr_b16 a[202:203], v230 offset:45056\n\t"
    "v_cvt_pk_bf16_f32 v208, v144, v145\n\t"
    "v_and_b32 v240, 15, v255\n\t"
    "v_lshrrev_b32 v241, 4, v255\n\t"
    "v_and_b32 v242, 1, v241\n\t"
    "v_mfma_f32_16x16x32_bf16 v[128:131], a[168:171], a[72:75], v[128:131]\n\t"
    "v_exp_f32 v146, v146\n\t"
    "v_mfma_f32_16x16x32_bf16 v[128:131], a[172:175], a[76:79], v[128:131]\n\t"
    "ds_read_b64_tr_b16 a[204:205], v231 offset:40960\n\t"
    "v_lshrrev_b32 v243, 1, v241\n\t"
    "v_lshl_add_u32 v242, v242, 1, v243\n\t"
    "v_and_b32 v243, 7, v240\n\t"
    "v_mfma_f32_16x16x32_bf16 v[132:135], a[176:179], a[64:67], v[176:179]\n\t"
    "v_exp_f32 v147, v147\n\t"
    "v_mfma_f32_16x16x32_bf16 v[132:135], a[180:183], a[68:71], v[132:135]\n\t"
    "ds_read_b64_tr_b16 a[206:207], v231 offset:45056\n\t"
    "v_cvt_pk_bf16_f32 v209, v146, v147\n\t"
    "v_xor_b32 v242, v242, v243\n\t"
    "v_lshlrev_b32 v243, 8, v240\n\t"
    "v_lshl_add_u32 v242, v242, 4, v243\n\t"
    "v_mfma_f32_16x16x32_bf16 v[132:135], a[184:187], a[72:75], v[132:135]\n\t"
    "v_exp_f32 v148, v148\n\t"
    "v_mfma_f32_16x16x32_bf16 v[132:135], a[188:191], a[76:79], v[132:135]\n\t"
    "ds_read_b64_tr_b16 a[208:209], v232 offset:40960\n\t"
    "s_lshl_b32 s83, %[w], 12\n\t"
    "s_add_u32 s83, s83, %[lds]\n\t"
    "s_add_u32 s83, s83, 131072\n\t"
    "v_mfma_f32_16x16x32_bf16 v[136:139], a[160:163], a[80:83], v[180:183]\n\t"
    "v_exp_f32 v149, v149\n\t"
    "v_mfma_f32_16x16x32_bf16 v[136:139], a[164:167], a[84:87], v[136:139]\n\t"
    "ds_read_b64_tr_b16 a[210:211], v232 offset:45056\n\t"
    "v_cvt_pk_bf16_f32 v210, v148, v149\n\t"
    "v_add_u32 v246, s83, v242\n\t"
    "v_xor_b32 v247, 64, v246\n\t"
    "v_xor_b32 v248, 128, v246\n\t"
    "v_mfma_f32_16x16x32_bf16 v[136:139], a[168:171], a[88:91], v[136:139]\n\t"
    "v_exp_f32 v150, v150\n\t"
    "v_mfma_f32_16x16x32_bf16 v[136:139], a[172:175], a[92:95], v[136:139]\n\t"
    "ds_read_b64_tr_b16 a[212:213], v233 offset:40960\n\t"
    "v_xor_b32 v249, 192, v246\n\t"
    "v_lshrrev_b32 v241, 4, v255\n\t"
    "v_and_b32 v240, 15, v255\n\t"
    "v_mfma_f32_16x16x32_bf16 v[140:143], a[176:179], a[80:83], v[180:183]\n\t"
    "v_exp_f32 v151, v151\n\t"
    "v_mfma_f32_16x16x32_bf16 v[140:143], a[180:183], a[84:87], v[140:143]\n\t"
    "ds_read_b64_tr_b16 a[214:215], v233 offset:45056\n\t"
    "v_cvt_pk_bf16_f32 v211, v150, v151\n\t"
    "v_xor_b32 v240, v240, v241\n\t"
    "v_lshlrev_b32 v241, 8, v241\n\t"
    "v_lshl_add_u32 v240, v240, 4, v241\n\t"
    "v_mfma_f32_16x16x32_bf16 v[140:143], a[184:187], a[88:91], v[140:143]\n\t"
    "v_exp_f32 v152, v152\n\t"
    "v_mfma_f32_16x16x32_bf16 v[140:143], a[188:191], a[92:95], v[140:143]\n\t"
    "ds_read_b64_tr_b16 a[216:217], v234 offset:40960\n\t"
    "v_add_u32 v236, s83, v240\n\t"
    "v_xor_b32 v237, 64, v236\n\t"
    "v_lshlrev_b32 v238, 4, v255\n\t"
    "v_mfma_f32_16x16x32_bf16 v[0:3], a[192:195], v[200:203], v[0:3]\n\t"
    "v_exp_f32 v153, v153\n\t"
    "v_mfma_f32_16x16x32_bf16 v[32:35], a[192:195], v[204:207], v[32:35]\n\t"
    "ds_read_b64_tr_b16 a[218:219], v234 offset:45056\n\t"
    "v_cvt_pk_bf16_f32 v212, v152, v153\n\t"
    "s_waitcnt lgkmcnt(2)\n\t"
    "v_mfma_f32_16x16x32_bf16 v[4:7], a[196:199], v[200:203], v[4:7]\n\t"
    "v_exp_f32 v154, v154\n\t"
    "v_mfma_f32_16x16x32_bf16 v[36:39], a[196:199], v[204:207], v[36:39]\n\t"
    "ds_read_b64_tr_b16 a[220:221], v235 offset:40960\n\t"
    "v_mfma_f32_16x16x32_bf16 v[8:11], a[200:203], v[200:203], v[8:11]\n\t"
    "v_exp_f32 v155, v155\n\t"
    "v_mfma_f32_16x16x32_bf16 v[40:43], a[200:203], v[204:207], v[40:43]\n\t"
    "ds_read_b64_tr_b16 a[222:223], v235 offset:45056\n\t"
    "v_cvt_pk_bf16_f32 v213, v154, v155\n\t"
    "v_mfma_f32_16x16x32_bf16 v[12:15], a[204:207], v[200:203], v[12:15]\n\t"
    "v_exp_f32 v156, v156\n\t"
    "v_mfma_f32_16x16x32_bf16 v[44:47], a[204:207], v[204:207], v[44:47]\n\t"
    "ds_read_b128 a[160:163], v224 offset:57344\n\t"
    "v_mfma_f32_16x16x32_bf16 v[16:19], a[208:211], v[200:203], v[16:19]\n\t"
    "v_exp_f32 v157, v157\n\t"
    "v_mfma_f32_16x16x32_bf16 v[48:51], a[208:211], v[204:207], v[48:51]\n\t"
    "ds_read_b128 a[164:167], v225 offset:57344\n\t"
    "v_cvt_pk_bf16_f32 v214, v156, v157\n\t"
    "v_mfma_f32_16x16x32_bf16 v[20:23], a[212:215], v[200:203], v[20:23]\n\t"
    "v_exp_f32 v158, v158\n\t"
    "v_mfma_f32_16x16x32_bf16 v[52:55], a[212:215], v[204:207], v[52:55]\n\t"
    "ds_read_b128 a[168:171], v226 offset:57344\n\t"
    "s_waitcnt lgkmcnt(2)\n\t"
    "v_mfma_f32_16x16x32_bf16 v[24:27], a[216:219], v[200:203], v[24:27]\n\t"
    "v_exp_f32 v159, v159\n\t"
    "v_mfma_f32_16x16x32_bf16 v[56:59], a[216:219], v[204:207], v[56:59]\n\t"
    "ds_read_b128 a[172:175], v227 offset:57344\n\t"
    "v_cvt_pk_bf16_f32 v215, v158, v159\n\t"
    "v_mfma_f32_16x16x32_bf16 v[28:31], a[220:223], v[200:203], v[28:31]\n\t"
    "v_mfma_f32_16x16x32_bf16 v[60:63], a[220:223], v[204:207], v[60:63]\n\t"
    "ds_read_b128 a[176:179], v224 offset:61440\n\t"
    "v_mfma_f32_16x16x32_bf16 a[240:243], v[196:199], v[200:203], a[240:243]\n\t"
    "v_exp_f32 v128, v128\n\t"
    "v_mfma_f32_16x16x32_bf16 a[244:247], v[196:199], v[204:207], a[244:247]\n\t"
    "ds_read_b128 a[180:183], v225 offset:61440\n\t"
    "s_waitcnt vmcnt(4)\n\t"
    "s_barrier\n\t"
    "v_mfma_f32_16x16x32_bf16 v[64:67], a[192:195], v[208:211], v[64:67]\n\t"
    "v_exp_f32 v129, v129\n\t"
    "v_mfma_f32_16x16x32_bf16 v[96:99], a[192:195], v[212:215], v[96:99]\n\t"
    "ds_read_b128 a[184:187], v226 offset:61440\n\t"
    "v_cvt_pk_bf16_f32 v200, v128, v129\n\t"
    "v_mfma_f32_16x16x32_bf16 v[68:71], a[196:199], v[208:211], v[68:71]\n\t"
    "v_exp_f32 v130, v130\n\t"
    "v_mfma_f32_16x16x32_bf16 v[100:103], a[196:199], v[212:215], v[100:103]\n\t"
    "ds_read_b128 a[188:191], v227 offset:61440\n\t"
    "v_mfma_f32_16x16x32_bf16 v[72:75], a[200:203], v[208:211], v[72:75]\n\t"
    "v_exp_f32 v131, v131\n\t"
    "v_mfma_f32_16x16x32_bf16 v[104:107], a[200:203], v[212:215], v[104:107]\n\t"
    "ds_read_b64_tr_b16 a[192:193], v228 offset:49152\n\t"
    "v_cvt_pk_bf16_f32 v201, v130, v131\n\t"
    "v_mfma_f32_16x16x32_bf16 v[76:79], a[204:207], v[208:211], v[76:79]\n\t"
    "v_exp_f32 v132, v132\n\t"
    "v_mfma_f32_16x16x32_bf16 v[108:111], a[204:207], v[212:215], v[108:111]\n\t"
    "ds_read_b64_tr_b16 a[194:195], v228 offset:53248\n\t"
    "v_mfma_f32_16x16x32_bf16 v[80:83], a[208:211], v[208:211], v[80:83]\n\t"
    "v_exp_f32 v133, v133\n\t"
    "v_mfma_f32_16x16x32_bf16 v[112:115], a[208:211], v[212:215], v[112:115]\n\t"
    "ds_read_b64_tr_b16 a[196:197], v229 offset:49152\n\t"
    "v_cvt_pk_bf16_f32 v202, v132, v133\n\t"
    "v_mfma_f32_16x16x32_bf16 v[84:87], a[212:215], v[208:211], v[84:87]\n\t"
    "v_exp_f32 v134, v134\n\t"
    "v_mfma_f32_16x16x32_bf16 v[116:119], a[212:215], v[212:215], v[116:119]\n\t"
    "ds_read_b64_tr_b16 a[198:199], v229 offset:53248\n\t"
    "v_mfma_f32_16x16x32_bf16 v[88:91], a[216:219], v[208:211], v[88:91]\n\t"
    "v_exp_f32 v135, v135\n\t"
    "v_mfma_f32_16x16x32_bf16 v[120:123], a[216:219], v[212:215], v[120:123]\n\t"
    "ds_read_b64_tr_b16 a[200:201], v230 offset:49152\n\t"
    "v_cvt_pk_bf16_f32 v203, v134, v135\n\t"
    "v_mfma_f32_16x16x32_bf16 v[92:95], a[220:223], v[208:211], v[92:95]\n\t"
    "v_exp_f32 v136, v136\n\t"
    "v_mfma_f32_16x16x32_bf16 v[124:127], a[220:223], v[212:215], v[124:127]\n\t"
    "ds_read_b64_tr_b16 a[202:203], v230 offset:53248\n\t"
    "v_mfma_f32_16x16x32_bf16 a[248:251], v[196:199], v[208:211], a[248:251]\n\t"
    "v_exp_f32 v137, v137\n\t"
    "v_mfma_f32_16x16x32_bf16 a[252:255], v[196:199], v[212:215], a[252:255]\n\t"
    "ds_read_b64_tr_b16 a[204:205], v231 offset:49152\n\t"
    "v_cvt_pk_bf16_f32 v204, v136, v137\n\t"
    "v_mfma_f32_16x16x32_bf16 v[144:147], a[160:163], a[64:67], v[176:179]\n\t"
    "v_exp_f32 v138, v138\n\t"
    "s_waitcnt lgkmcnt(2)\n\t"
    "v_mfma_f32_16x16x32_bf16 v[144:147], a[164:167], a[68:71], v[144:147]\n\t"
    "v_add_u32 v240, s63, v224\n\t"
    "ds_read_b128 a[224:227], v240 offset:32768\n\t"
    "v_mfma_f32_16x16x32_bf16 v[144:147], a[168:171], a[72:75], v[144:147]\n\t"
    "v_exp_f32 v139, v139\n\t"
    "v_mfma_f32_16x16x32_bf16 v[144:147], a[172:175], a[76:79], v[144:147]\n\t"
    "v_add_u32 v241, s63, v225\n\t"
    "ds_read_b128 a[228:231], v241 offset:32768\n\t"
    "v_cvt_pk_bf16_f32 v205, v138, v139\n\t"
    "v_mfma_f32_16x16x32_bf16 v[148:151], a[176:179], a[64:67], v[176:179]\n\t"
    "v_exp_f32 v140, v140\n\t"
    "v_mfma_f32_16x16x32_bf16 v[148:151], a[180:183], a[68:71], v[148:151]\n\t"
    "v_add_u32 v242, s63, v226\n\t"
    "ds_read_b128 a[232:235], v242 offset:32768\n\t"
    "v_mfma_f32_16x16x32_bf16 v[148:151], a[184:187], a[72:75], v[148:151]\n\t"
    "v_exp_f32 v141, v141\n\t"
    "v_mfma_f32_16x16x32_bf16 v[148:151], a[188:191], a[76:79], v[148:151]\n\t"
    "v_add_u32 v243, s63, v227\n\t"
    "ds_read_b128 a[236:239], v243 offset:32768\n\t"
    "v_cvt_pk_bf16_f32 v206, v140, v141\n\t"
    "v_mfma_f32_16x16x32_bf16 v[152:155], a[160:163], a[80:83], v[180:183]\n\t"
    "v_exp_f32 v142, v142\n\t"
    "v_mfma_f32_16x16x32_bf16 v[152:155], a[164:167], a[84:87], v[152:155]\n\t"
    "ds_read_b64_tr_b16 a[206:207], v231 offset:53248\n\t"
    "v_mfma_f32_16x16x32_bf16 v[152:155], a[168:171], a[88:91], v[152:155]\n\t"
    "v_exp_f32 v143, v143\n\t"
    "v_mfma_f32_16x16x32_bf16 v[152:155], a[172:175], a[92:95], v[152:155]\n\t"
    "ds_read_b64_tr_b16 a[208:209], v232 offset:49152\n\t"
    "v_cvt_pk_bf16_f32 v207, v142, v143\n\t"
    "v_mfma_f32_16x16x32_bf16 v[156:159], a[176:179], a[80:83], v[180:183]\n\t"
    "v_mfma_f32_16x16x32_bf16 v[156:159], a[180:183], a[84:87], v[156:159]\n\t"
    "ds_read_b64_tr_b16 a[210:211], v232 offset:53248\n\t"
    "v_mfma_f32_16x16x32_bf16 v[156:159], a[184:187], a[88:91], v[156:159]\n\t"
    "v_mfma_f32_16x16x32_bf16 v[156:159], a[188:191], a[92:95], v[156:159]\n\t"
    "ds_read_b64_tr_b16 a[212:213], v233 offset:49152\n\t"
    "s_waitcnt lgkmcnt(2)\n\t"
    "v_mfma_f32_16x16x32_bf16 v[160:163], a[224:227], a[128:131], 0\n\t"
    "v_mfma_f32_16x16x32_bf16 v[160:163], a[228:231], a[132:135], v[160:163]\n\t"
    "ds_read_b64_tr_b16 a[214:215], v233 offset:53248\n\t"
    "v_mfma_f32_16x16x32_bf16 v[160:163], a[232:235], a[136:139], v[160:163]\n\t"
    "v_exp_f32 v144, v144\n\t"
    "v_mfma_f32_16x16x32_bf16 v[160:163], a[236:239], a[140:143], v[160:163]\n\t"
    "ds_read_b64_tr_b16 a[216:217], v234 offset:49152\n\t"
    "v_mfma_f32_16x16x32_bf16 v[168:171], a[224:227], a[144:147], 0\n\t"
    "v_exp_f32 v145, v145\n\t"
    "v_mfma_f32_16x16x32_bf16 v[168:171], a[228:231], a[148:151], v[168:171]\n\t"
    "ds_read_b64_tr_b16 a[218:219], v234 offset:53248\n\t"
    "v_cvt_pk_bf16_f32 v208, v144, v145\n\t"
    "v_mfma_f32_16x16x32_bf16 v[168:171], a[232:235], a[152:155], v[168:171]\n\t"
    "v_exp_f32 v146, v146\n\t"
    "v_mfma_f32_16x16x32_bf16 v[168:171], a[236:239], a[156:159], v[168:171]\n\t"
    "ds_read_b64_tr_b16 a[220:221], v235 offset:49152\n\t"
    "s_waitcnt vmcnt(0)\n\t"
    "s_barrier\n\t"
    "v_mfma_f32_16x16x32_bf16 v[0:3], a[192:195], v[200:203], v[0:3]\n\t"
    "ds_read_b64_tr_b16 a[222:223], v235 offset:53248\n\t"
    "v_exp_f32 v147, v147\n\t"
    "v_mfma_f32_16x16x32_bf16 v[32:35], a[192:195], v[204:207], v[32:35]\n\t"
    "ds_read_b128 a[224:227], v240 offset:36864\n\t"
    "v_cvt_pk_bf16_f32 v209, v146, v147\n\t"
    "v_mfma_f32_16x16x32_bf16 v[4:7], a[196:199], v[200:203], v[4:7]\n\t"
    "ds_read_b128 a[228:231], v241 offset:36864\n\t"
    "v_exp_f32 v148, v148\n\t"
    "v_mfma_f32_16x16x32_bf16 v[36:39], a[196:199], v[204:207], v[36:39]\n\t"
    "ds_read_b128 a[232:235], v242 offset:36864\n\t"
    "v_mfma_f32_16x16x32_bf16 v[8:11], a[200:203], v[200:203], v[8:11]\n\t"
    "ds_read_b128 a[236:239], v243 offset:36864\n\t"
    "v_exp_f32 v149, v149\n\t"
    "v_mfma_f32_16x16x32_bf16 v[40:43], a[200:203], v[204:207], v[40:43]\n\t"
    "ds_read_b128 a[160:163], v224 offset:49152\n\t"
    "v_cvt_pk_bf16_f32 v210, v148, v149\n\t"
    "v_mfma_f32_16x16x32_bf16 v[12:15], a[204:207], v[200:203], v[12:15]\n\t"
    "v_exp_f32 v150, v150\n\t"
    "v_mfma_f32_16x16x32_bf16 v[44:47], a[204:207], v[204:207], v[44:47]\n\t"
    "ds_read_b128 a[164:167], v225 offset:49152\n\t"
    "s_waitcnt lgkmcnt(3)\n\t"
    "v_mfma_f32_16x16x32_bf16 v[16:19], a[208:211], v[200:203], v[16:19]\n\t"
    "v_exp_f32 v151, v151\n\t"
    "v_mfma_f32_16x16x32_bf16 v[48:51], a[208:211], v[204:207], v[48:51]\n\t"
    "ds_read_b128 a[168:171], v226 offset:49152\n\t"
    "v_cvt_pk_bf16_f32 v211, v150, v151\n\t"
    "v_mfma_f32_16x16x32_bf16 v[20:23], a[212:215], v[200:203], v[20:23]\n\t"
    "v_exp_f32 v152, v152\n\t"
    "v_mfma_f32_16x16x32_bf16 v[52:55], a[212:215], v[204:207], v[52:55]\n\t"
    "ds_read_b128 a[172:175], v227 offset:49152\n\t"
    "v_mfma_f32_16x16x32_bf16 v[24:27], a[216:219], v[200:203], v[24:27]\n\t"
    "v_exp_f32 v153, v153\n\t"
    "v_mfma_f32_16x16x32_bf16 v[56:59], a[216:219], v[204:207], v[56:59]\n\t"
    "ds_read_b128 a[176:179], v224 offset:53248\n\t"
    "v_cvt_pk_bf16_f32 v212, v152, v153\n\t"
    "v_mfma_f32_16x16x32_bf16 v[28:31], a[220:223], v[200:203], v[28:31]\n\t"
    "v_exp_f32 v154, v154\n\t"
    "v_mfma_f32_16x16x32_bf16 v[60:63], a[220:223], v[204:207], v[60:63]\n\t"
    "ds_read_b128 a[180:183], v225 offset:53248\n\t"
    "v_mfma_f32_16x16x32_bf16 a[240:243], v[196:199], v[200:203], a[240:243]\n\t"
    "v_exp_f32 v155, v155\n\t"
    "v_mfma_f32_16x16x32_bf16 a[244:247], v[196:199], v[204:207], a[244:247]\n\t"
    "ds_read_b128 a[184:187], v226 offset:53248\n\t"
    "v_cvt_pk_bf16_f32 v213, v154, v155\n\t"
    "v_mfma_f32_16x16x32_bf16 v[164:167], a[224:227], a[128:131], 0\n\t"
    "ds_read_b128 a[188:191], v227 offset:53248\n\t"
    "v_exp_f32 v156, v156\n\t"
    "v_mfma_f32_16x16x32_bf16 v[164:167], a[228:231], a[132:135], v[164:167]\n\t"
    "ds_read_b64_tr_b16 a[192:193], v228 offset:57344\n\t"
    "v_mfma_f32_16x16x32_bf16 v[164:167], a[232:235], a[136:139], v[164:167]\n\t"
    "ds_read_b64_tr_b16 a[194:195], v228 offset:61440\n\t"
    "v_exp_f32 v157, v157\n\t"
    "s_waitcnt lgkmcnt(4)\n\t"
    "v_mfma_f32_16x16x32_bf16 v[164:167], a[236:239], a[140:143], v[164:167]\n\t"
    "ds_read_b64_tr_b16 a[196:197], v229 offset:57344\n\t"
    "v_cvt_pk_bf16_f32 v214, v156, v157\n\t"
    "v_mfma_f32_16x16x32_bf16 v[172:175], a[224:227], a[144:147], 0\n\t"
    "ds_read_b64_tr_b16 a[198:199], v229 offset:61440\n\t"
    "v_exp_f32 v158, v158\n\t"
    "v_mfma_f32_16x16x32_bf16 v[172:175], a[228:231], a[148:151], v[172:175]\n\t"
    "ds_read_b64_tr_b16 a[200:201], v230 offset:57344\n\t"
    "v_mfma_f32_16x16x32_bf16 v[172:175], a[232:235], a[152:155], v[172:175]\n\t"
    "v_exp_f32 v159, v159\n\t"
    "v_mfma_f32_16x16x32_bf16 v[172:175], a[236:239], a[156:159], v[172:175]\n\t"
    "ds_read_b64_tr_b16 a[202:203], v230 offset:61440\n\t"
    "v_cvt_pk_bf16_f32 v215, v158, v159\n\t"
    "v_mfma_f32_16x16x32_bf16 v[128:131], a[160:163], a[96:99], v[184:187]\n\t"
    "ds_read_b64_tr_b16 a[204:205], v231 offset:57344\n\t"
    "v_mfma_f32_16x16x32_bf16 v[128:131], a[164:167], a[100:103], v[128:131]\n\t"
    "ds_read_b64_tr_b16 a[206:207], v231 offset:61440\n\t"
    "v_mfma_f32_16x16x32_bf16 v[128:131], a[168:171], a[104:107], v[128:131]\n\t"
    "ds_read_b64_tr_b16 a[208:209], v232 offset:57344\n\t"
    "v_fma_f32 v160, v160, s84, v192\n\t"
    "v_exp_f32 v160, v160\n\t"
    "v_mfma_f32_16x16x32_bf16 v[128:131], a[172:175], a[108:111], v[128:131]\n\t"
    "ds_read_b64_tr_b16 a[210:211], v232 offset:61440\n\t"
    "v_fma_f32 v161, v161, s84, v192\n\t"
    "v_exp_f32 v161, v161\n\t"
    "v_mfma_f32_16x16x32_bf16 v[132:135], a[176:179], a[96:99], v[184:187]\n\t"
    "v_fma_f32 v162, v162, s84, v192\n\t"
    "v_exp_f32 v162, v162\n\t"
    "v_mfma_f32_16x16x32_bf16 v[132:135], a[180:183], a[100:103], v[132:135]\n\t"
    "ds_read_b64_tr_b16 a[212:213], v233 offset:57344\n\t"
    "v_fma_f32 v163, v163, s84, v192\n\t"
    "v_exp_f32 v163, v163\n\t"
    "s_waitcnt lgkmcnt(3)\n\t"
    "v_mfma_f32_16x16x32_bf16 v[132:135], a[184:187], a[104:107], v[132:135]\n\t"
    "v_fma_f32 v164, v164, s84, v192\n\t"
    "v_exp_f32 v164, v164\n\t"
    "v_mfma_f32_16x16x32_bf16 v[132:135], a[188:191], a[108:111], v[132:135]\n\t"
    "ds_read_b64_tr_b16 a[214:215], v233 offset:61440\n\t"
    "v_cvt_pk_bf16_f32 v160, v160, v161\n\t"
    "v_cvt_pk_bf16_f32 v161, v162, v163\n\t"
    "v_mfma_f32_16x16x32_bf16 v[136:139], a[160:163], a[112:115], v[188:191]\n\t"
    "v_fma_f32 v165, v165, s84, v192\n\t"
    "v_exp_f32 v165, v165\n\t"
    "v_mfma_f32_16x16x32_bf16 v[136:139], a[164:167], a[116:119], v[136:139]\n\t"
    "ds_read_b64_tr_b16 a[216:217], v234 offset:57344\n\t"
    "v_cvt_pk_bf16_f32 v162, v164, v165\n\t"
    "v_mfma_f32_16x16x32_bf16 v[136:139], a[168:171], a[120:123], v[136:139]\n\t"
    "v_fma_f32 v166, v166, s84, v192\n\t"
    "v_exp_f32 v166, v166\n\t"
    "v_mfma_f32_16x16x32_bf16 v[136:139], a[172:175], a[124:127], v[136:139]\n\t"
    "ds_read_b64_tr_b16 a[218:219], v234 offset:61440\n\t"
    "v_mfma_f32_16x16x32_bf16 v[140:143], a[176:179], a[112:115], v[188:191]\n\t"
    "v_fma_f32 v167, v167, s84, v192\n\t"
    "v_exp_f32 v167, v167\n\t"
    "v_mfma_f32_16x16x32_bf16 v[140:143], a[180:183], a[116:119], v[140:143]\n\t"
    "ds_read_b64_tr_b16 a[220:221], v235 offset:57344\n\t"
    "v_cvt_pk_bf16_f32 v163, v166, v167\n\t"
    "v_mfma_f32_16x16x32_bf16 v[140:143], a[184:187], a[120:123], v[140:143]\n\t"
    "v_fma_f32 v168, v168, s84, v193\n\t"
    "v_exp_f32 v168, v168\n\t"
    "v_mfma_f32_16x16x32_bf16 v[140:143], a[188:191], a[124:127], v[140:143]\n\t"
    "ds_read_b64_tr_b16 a[222:223], v235 offset:61440\n\t"
    "v_mfma_f32_16x16x32_bf16 v[0:3], a[192:195], v[208:211], v[0:3]\n\t"
    "v_add_u32 v176, s63, v228\n\t"
    "ds_read_b64_tr_b16 a[224:225], v176 offset:32768\n\t"
    "v_fma_f32 v169, v169, s84, v193\n\t"
    "v_exp_f32 v169, v169\n\t"
    "v_mfma_f32_16x16x32_bf16 v[32:35], a[192:195], v[212:215], v[32:35]\n\t"
    "ds_read_b64_tr_b16 a[226:227], v176 offset:36864\n\t"
    "v_cvt_pk_bf16_f32 v168, v168, v169\n\t"
    "v_mfma_f32_16x16x32_bf16 v[4:7], a[196:199], v[208:211], v[4:7]\n\t"
    "v_fma_f32 v170, v170, s84, v193\n\t"
    "v_exp_f32 v170, v170\n\t"
    "v_mfma_f32_16x16x32_bf16 v[36:39], a[196:199], v[212:215], v[36:39]\n\t"
    "v_add_u32 v177, s63, v229\n\t"
    "ds_read_b64_tr_b16 a[228:229], v177 offset:32768\n\t"
    "v_mfma_f32_16x16x32_bf16 v[8:11], a[200:203], v[208:211], v[8:11]\n\t"
    "v_fma_f32 v171, v171, s84, v193\n\t"
    "v_exp_f32 v171, v171\n\t"
    "v_mfma_f32_16x16x32_bf16 v[40:43], a[200:203], v[212:215], v[40:43]\n\t"
    "ds_read_b64_tr_b16 a[230:231], v177 offset:36864\n\t"
    "v_cvt_pk_bf16_f32 v169, v170, v171\n\t"
    "v_mfma_f32_16x16x32_bf16 v[12:15], a[204:207], v[208:211], v[12:15]\n\t"
    "v_fma_f32 v172, v172, s84, v193\n\t"
    "v_exp_f32 v172, v172\n\t"
    "v_mfma_f32_16x16x32_bf16 v[44:47], a[204:207], v[212:215], v[44:47]\n\t"
    "v_add_u32 v178, s63, v230\n\t"
    "ds_read_b64_tr_b16 a[232:233], v178 offset:32768\n\t"
    "s_waitcnt lgkmcnt(2)\n\t"
    "v_mfma_f32_16x16x32_bf16 v[16:19], a[208:211], v[208:211], v[16:19]\n\t"
    "v_fma_f32 v173, v173, s84, v193\n\t"
    "v_exp_f32 v173, v173\n\t"
    "v_mfma_f32_16x16x32_bf16 v[48:51], a[208:211], v[212:215], v[48:51]\n\t"
    "ds_read_b64_tr_b16 a[234:235], v178 offset:36864\n\t"
    "v_cvt_pk_bf16_f32 v170, v172, v173\n\t"
    "v_mfma_f32_16x16x32_bf16 v[20:23], a[212:215], v[208:211], v[20:23]\n\t"
    "v_fma_f32 v174, v174, s84, v193\n\t"
    "v_exp_f32 v174, v174\n\t"
    "v_mfma_f32_16x16x32_bf16 v[52:55], a[212:215], v[212:215], v[52:55]\n\t"
    "v_add_u32 v179, s63, v231\n\t"
    "ds_read_b64_tr_b16 a[236:237], v179 offset:32768\n\t"
    "v_mfma_f32_16x16x32_bf16 v[24:27], a[216:219], v[208:211], v[24:27]\n\t"
    "v_fma_f32 v175, v175, s84, v193\n\t"
    "v_exp_f32 v175, v175\n\t"
    "v_mfma_f32_16x16x32_bf16 v[56:59], a[216:219], v[212:215], v[56:59]\n\t"
    "ds_read_b64_tr_b16 a[238:239], v179 offset:36864\n\t"
    "v_cvt_pk_bf16_f32 v171, v174, v175\n\t"
    "v_mfma_f32_16x16x32_bf16 v[28:31], a[220:223], v[208:211], v[28:31]\n\t"
    "v_exp_f32 v128, v128\n\t"
    "v_mfma_f32_16x16x32_bf16 v[60:63], a[220:223], v[212:215], v[60:63]\n\t"
    "ds_read_b128 a[160:163], v224 offset:57344\n\t"
    "v_mfma_f32_16x16x32_bf16 a[240:243], v[196:199], v[208:211], a[240:243]\n\t"
    "v_exp_f32 v129, v129\n\t"
    "v_mfma_f32_16x16x32_bf16 a[244:247], v[196:199], v[212:215], a[244:247]\n\t"
    "ds_read_b128 a[164:167], v225 offset:57344\n\t"
    "v_cvt_pk_bf16_f32 v200, v128, v129\n\t"
    "v_mfma_f32_16x16x32_bf16 a[0:3], a[224:227], v[160:163], a[0:3]\n\t"
    "ds_read_b128 a[168:171], v226 offset:57344\n\t"
    "v_exp_f32 v130, v130\n\t"
    "v_mfma_f32_16x16x32_bf16 a[32:35], a[224:227], v[168:171], a[32:35]\n\t"
    "ds_read_b128 a[172:175], v227 offset:57344\n\t"
    "v_accvgpr_read_b32 v244, a240\n\t"
    "v_accvgpr_read_b32 v245, a244\n\t"
    "v_rcp_f32 v250, v244\n\t"
    "s_waitcnt lgkmcnt(3)\n\t"
    "v_mfma_f32_16x16x32_bf16 a[4:7], a[228:231], v[160:163], a[4:7]\n\t"
    "ds_read_b128 a[176:179], v224 offset:61440\n\t"
    "v_exp_f32 v131, v131\n\t"
    "v_mfma_f32_16x16x32_bf16 a[36:39], a[228:231], v[168:171], a[36:39]\n\t"
    "ds_read_b128 a[180:183], v225 offset:61440\n\t"
    "v_cvt_pk_bf16_f32 v201, v130, v131\n\t"
    "v_rcp_f32 v252, v245\n\t"
    "v_cmp_ngt_f32_e64 s[94:95], s87, v244\n\t"
    "v_mfma_f32_16x16x32_bf16 a[8:11], a[232:235], v[160:163], a[8:11]\n\t"
    "ds_read_b128 a[184:187], v226 offset:61440\n\t"
    "v_exp_f32 v132, v132\n\t"
    "v_mfma_f32_16x16x32_bf16 a[40:43], a[232:235], v[168:171], a[40:43]\n\t"
    "ds_read_b128 a[188:191], v227 offset:61440\n\t"
    "s_or_b64 s[88:89], s[88:89], s[94:95]\n\t"
    "v_cmp_ngt_f32_e64 s[94:95], s87, v245\n\t"
    "v_mfma_f32_16x16x32_bf16 a[12:15], a[236:239], v[160:163], a[12:15]\n\t"
    "v_add_u32 v176, s63, v232\n\t"
    "ds_read_b64_tr_b16 a[224:225], v176 offset:32768\n\t"
    "v_exp_f32 v133, v133\n\t"
    "v_mfma_f32_16x16x32_bf16 a[44:47], a[236:239], v[168:171], a[44:47]\n\t"
    "ds_read_b64_tr_b16 a[226:227], v176 offset:36864\n\t"
    "v_cvt_pk_bf16_f32 v202, v132, v133\n\t"
    "s_or_b64 s[88:89], s[88:89], s[94:95]\n\t"
    "s_add_u32 s90, s82, 0\n\t"
    "s_add_u32 s91, s82, 4096\n\t"
    "v_mfma_f32_16x16x32_bf16 v[216:219], v[196:199], v[160:163], v[216:219]\n\t"
    "v_add_u32 v177, s63, v233\n\t"
    "ds_read_b64_tr_b16 a[228:229], v177 offset:32768\n\t"
    "v_exp_f32 v134, v134\n\t"
    "v_mfma_f32_16x16x32_bf16 v[220:223], v[196:199], v[168:171], v[220:223]\n\t"
    "ds_read_b64_tr_b16 a[230:231], v177 offset:36864\n\t"
    "v_pk_mul_f32 v[0:1], v[0:1], v[250:251] op_sel_hi:[1,0]\n\t"
    "v_pk_mul_f32 v[2:3], v[2:3], v[250:251] op_sel_hi:[1,0]\n\t"
    "v_pk_mul_f32 v[4:5], v[4:5], v[250:251] op_sel_hi:[1,0]\n\t"
    "v_mfma_f32_16x16x32_bf16 v[144:147], a[160:163], a[96:99], v[184:187]\n\t"
    "v_add_u32 v178, s63, v234\n\t"
    "ds_read_b64_tr_b16 a[232:233], v178 offset:32768\n\t"
    "v_exp_f32 v135, v135\n\t"
    "s_waitcnt lgkmcnt(4)\n\t"
    "v_mfma_f32_16x16x32_bf16 v[144:147], a[164:167], a[100:103], v[144:147]\n\t"
    "ds_read_b64_tr_b16 a[234:235], v178 offset:36864\n\t"
    "v_cvt_pk_bf16_f32 v203, v134, v135\n\t"
    "v_pk_mul_f32 v[6:7], v[6:7], v[250:251] op_sel_hi:[1,0]\n\t"
    "v_cvt_pk_bf16_f32 v0, v0, v1\n\t"
    "v_cvt_pk_bf16_f32 v1, v2, v3\n\t"
    "v_mfma_f32_16x16x32_bf16 v[144:147], a[168:171], a[104:107], v[144:147]\n\t"
    "v_add_u32 v179, s63, v235\n\t"
    "ds_read_b64_tr_b16 a[236:237], v179 offset:32768\n\t"
    "v_exp_f32 v136, v136\n\t"
    "v_mfma_f32_16x16x32_bf16 v[144:147], a[172:175], a[108:111], v[144:147]\n\t"
    "ds_read_b64_tr_b16 a[238:239], v179 offset:36864\n\t"
    "v_cvt_pk_bf16_f32 v2, v4, v5\n\t"
    "v_cvt_pk_bf16_f32 v3, v6, v7\n\t"
    "v_pk_mul_f32 v[8:9], v[8:9], v[250:251] op_sel_hi:[1,0]\n\t"
    "v_mfma_f32_16x16x32_bf16 v[148:151], a[176:179], a[96:99], v[184:187]\n\t"
    "ds_read_b64_tr_b16 a[192:193], v228 offset:49152\n\t"
    "v_exp_f32 v137, v137\n\t"
    "v_mfma_f32_16x16x32_bf16 v[148:151], a[180:183], a[100:103], v[148:151]\n\t"
    "ds_read_b64_tr_b16 a[194:195], v228 offset:53248\n\t"
    "v_cvt_pk_bf16_f32 v204, v136, v137\n\t"
    "v_pk_mul_f32 v[10:11], v[10:11], v[250:251] op_sel_hi:[1,0]\n\t"
    "v_pk_mul_f32 v[12:13], v[12:13], v[250:251] op_sel_hi:[1,0]\n\t"
    "v_pk_mul_f32 v[14:15], v[14:15], v[250:251] op_sel_hi:[1,0]\n\t"
    "v_mfma_f32_16x16x32_bf16 v[148:151], a[184:187], a[104:107], v[148:151]\n\t"
    "v_exp_f32 v138, v138\n\t"
    "v_mfma_f32_16x16x32_bf16 v[148:151], a[188:191], a[108:111], v[148:151]\n\t"
    "ds_read_b64_tr_b16 a[196:197], v229 offset:49152\n\t"
    "v_cvt_pk_bf16_f32 v8, v8, v9\n\t"
    "v_cvt_pk_bf16_f32 v9, v10, v11\n\t"
    "v_cvt_pk_bf16_f32 v10, v12, v13\n\t"
    "v_mfma_f32_16x16x32_bf16 v[152:155], a[160:163], a[112:115], v[188:191]\n\t"
    "v_exp_f32 v139, v139\n\t"
    "v_mfma_f32_16x16x32_bf16 v[152:155], a[164:167], a[116:119], v[152:155]\n\t"
    "ds_read_b64_tr_b16 a[198:199], v229 offset:53248\n\t"
    "v_cvt_pk_bf16_f32 v205, v138, v139\n\t"
    "v_cvt_pk_bf16_f32 v11, v14, v15\n\t"
    "v_pk_mul_f32 v[16:17], v[16:17], v[250:251] op_sel_hi:[1,0]\n\t"
    "v_pk_mul_f32 v[18:19], v[18:19], v[250:251] op_sel_hi:[1,0]\n\t"
    "v_mfma_f32_16x16x32_bf16 v[152:155], a[168:171], a[120:123], v[152:155]\n\t"
    "v_exp_f32 v140, v140\n\t"
    "v_mfma_f32_16x16x32_bf16 v[152:155], a[172:175], a[124:127], v[152:155]\n\t"
    "ds_read_b64_tr_b16 a[200:201], v230 offset:49152\n\t"
    "v_pk_mul_f32 v[20:21], v[20:21], v[250:251] op_sel_hi:[1,0]\n\t"
    "v_pk_mul_f32 v[22:23], v[22:23], v[250:251] op_sel_hi:[1,0]\n\t"
    "v_cvt_pk_bf16_f32 v16, v16, v17\n\t"
    "v_mfma_f32_16x16x32_bf16 v[156:159], a[176:179], a[112:115], v[188:191]\n\t"
    "v_exp_f32 v141, v141\n\t"
    "v_mfma_f32_16x16x32_bf16 v[156:159], a[180:183], a[116:119], v[156:159]\n\t"
    "ds_read_b64_tr_b16 a[202:203], v230 offset:53248\n\t"
    "v_cvt_pk_bf16_f32 v206, v140, v141\n\t"
    "v_cvt_pk_bf16_f32 v17, v18, v19\n\t"
    "v_cvt_pk_bf16_f32 v18, v20, v21\n\t"
    "v_cvt_pk_bf16_f32 v19, v22, v23\n\t"
    "v_mfma_f32_16x16x32_bf16 v[156:159], a[184:187], a[120:123], v[156:159]\n\t"
    "v_exp_f32 v142, v142\n\t"
    "v_mfma_f32_16x16x32_bf16 v[156:159], a[188:191], a[124:127], v[156:159]\n\t"
    "ds_read_b64_tr_b16 a[204:205], v231 offset:49152\n\t"
    "v_pk_mul_f32 v[24:25], v[24:25], v[250:251] op_sel_hi:[1,0]\n\t"
    "v_pk_mul_f32 v[26:27], v[26:27], v[250:251] op_sel_hi:[1,0]\n\t"
    "v_pk_mul_f32 v[28:29], v[28:29], v[250:251] op_sel_hi:[1,0]\n\t"
    "s_waitcnt lgkmcnt(2)\n\t"
    "v_mfma_f32_16x16x32_bf16 a[16:19], a[224:227], v[160:163], a[16:19]\n\t"
    "v_exp_f32 v143, v143\n\t"
    "v_mfma_f32_16x16x32_bf16 a[48:51], a[224:227], v[168:171], a[48:51]\n\t"
    "ds_read_b64_tr_b16 a[206:207], v231 offset:53248\n\t"
    "v_cvt_pk_bf16_f32 v207, v142, v143\n\t"
    "v_pk_mul_f32 v[30:31], v[30:31], v[250:251] op_sel_hi:[1,0]\n\t"
    "v_cvt_pk_bf16_f32 v24, v24, v25\n\t"
    "v_cvt_pk_bf16_f32 v25, v26, v27\n\t"
    "v_mfma_f32_16x16x32_bf16 a[20:23], a[228:231], v[160:163], a[20:23]\n\t"
    "v_exp_f32 v144, v144\n\t"
    "v_mfma_f32_16x16x32_bf16 a[52:55], a[228:231], v[168:171], a[52:55]\n\t"
    "ds_read_b64_tr_b16 a[208:209], v232 offset:49152\n\t"
    "v_exp_f32 v145, v145\n\t"
    "v_mfma_f32_16x16x32_bf16 a[24:27], a[232:235], v[160:163], a[24:27]\n\t"
    "v_exp_f32 v146, v146\n\t"
    "v_cvt_pk_bf16_f32 v208, v144, v145\n\t"
    "v_mfma_f32_16x16x32_bf16 a[56:59], a[232:235], v[168:171], a[56:59]\n\t"
    "ds_read_b64_tr_b16 a[210:211], v232 offset:53248\n\t"
    "v_exp_f32 v147, v147\n\t"
    "v_mfma_f32_16x16x32_bf16 a[28:31], a[236:239], v[160:163], a[28:31]\n\t"
    "v_exp_f32 v148, v148\n\t"
    "v_mfma_f32_16x16x32_bf16 a[60:63], a[236:239], v[168:171], a[60:63]\n\t"
    "ds_read_b64_tr_b16 a[212:213], v233 offset:49152\n\t"
    "v_exp_f32 v149, v149\n\t"
    "v_mfma_f32_16x16x32_bf16 v[64:67], a[192:195], v[200:203], v[64:67]\n\t"
    "ds_read_b64_tr_b16 a[214:215], v233 offset:53248\n\t"
    "v_exp_f32 v150, v150\n\t"
    "v_mfma_f32_16x16x32_bf16 v[96:99], a[192:195], v[204:207], v[96:99]\n\t"
    "ds_read_b64_tr_b16 a[216:217], v234 offset:49152\n\t"
    "v_cvt_pk_bf16_f32 v209, v146, v147\n\t"
    "v_cvt_pk_bf16_f32 v210, v148, v149\n\t"
    "v_cvt_pk_bf16_f32 v26, v28, v29\n\t"
    "v_cvt_pk_bf16_f32 v27, v30, v31\n\t"
    "v_permlane16_swap_b32 v0, v2\n\t"
    "v_mfma_f32_16x16x32_bf16 v[68:71], a[196:199], v[200:203], v[68:71]\n\t"
    "ds_read_b64_tr_b16 a[218:219], v234 offset:53248\n\t"
    "v_exp_f32 v151, v151\n\t"
    "v_mfma_f32_16x16x32_bf16 v[100:103], a[196:199], v[204:207], v[100:103]\n\t"
    "ds_read_b64_tr_b16 a[220:221], v235 offset:49152\n\t"
    "v_cvt_pk_bf16_f32 v211, v150, v151\n\t"
    "v_permlane16_swap_b32 v1, v3\n\t"
    "v_permlane16_swap_b32 v8, v10\n\t"
    "v_permlane16_swap_b32 v9, v11\n\t"
    "s_waitcnt lgkmcnt(4)\n\t"
    "v_mfma_f32_16x16x32_bf16 v[72:75], a[200:203], v[200:203], v[72:75]\n\t"
    "ds_read_b64_tr_b16 a[222:223], v235 offset:53248\n\t"
    "v_exp_f32 v152, v152\n\t"
    "v_mfma_f32_16x16x32_bf16 v[104:107], a[200:203], v[204:207], v[104:107]\n\t"
    "ds_read_b64_tr_b16 a[192:193], v228 offset:57344\n\t"
    "v_permlane16_swap_b32 v16, v18\n\t"
    "v_permlane16_swap_b32 v17, v19\n\t"
    "v_permlane16_swap_b32 v24, v26\n\t"
    "v_mfma_f32_16x16x32_bf16 v[76:79], a[204:207], v[200:203], v[76:79]\n\t"
    "ds_read_b64_tr_b16 a[194:195], v228 offset:61440\n\t"
    "v_exp_f32 v153, v153\n\t"
    "v_mfma_f32_16x16x32_bf16 v[108:111], a[204:207], v[204:207], v[108:111]\n\t"
    "ds_read_b64_tr_b16 a[196:197], v229 offset:57344\n\t"
    "v_cvt_pk_bf16_f32 v212, v152, v153\n\t"
    "v_permlane16_swap_b32 v25, v27\n\t"
    "ds_write_b128 v246, v[0:3] offset:0\n\t"
    "ds_write_b128 v247, v[8:11] offset:0\n\t"
    "v_mfma_f32_16x16x32_bf16 v[80:83], a[208:211], v[200:203], v[80:83]\n\t"
    "ds_read_b64_tr_b16 a[198:199], v229 offset:61440\n\t"
    "v_exp_f32 v154, v154\n\t"
    "v_mfma_f32_16x16x32_bf16 v[112:115], a[208:211], v[204:207], v[112:115]\n\t"
    "ds_read_b64_tr_b16 a[200:201], v230 offset:57344\n\t"
    "ds_write_b128 v248, v[16:19] offset:0\n\t"
    "ds_write_b128 v249, v[24:27] offset:0\n\t"
    "ds_read_b128 v[4:7], v236 offset:0\n\t"
    "s_waitcnt lgkmcnt(9)\n\t"
    "v_mfma_f32_16x16x32_bf16 v[84:87], a[212:215], v[200:203], v[84:87]\n\t"
    "ds_read_b64_tr_b16 a[202:203], v230 offset:61440\n\t"
    "v_exp_f32 v155, v155\n\t"
    "v_mfma_f32_16x16x32_bf16 v[116:119], a[212:215], v[204:207], v[116:119]\n\t"
    "ds_read_b64_tr_b16 a[204:205], v231 offset:57344\n\t"
    "v_cvt_pk_bf16_f32 v213, v154, v155\n\t"
    "ds_read_b128 v[12:15], v237 offset:1024\n\t"
    "ds_read_b128 v[20:23], v236 offset:2048\n\t"
    "ds_read_b128 v[28:31], v237 offset:3072\n\t"
    "v_mfma_f32_16x16x32_bf16 v[88:91], a[216:219], v[200:203], v[88:91]\n\t"
    "ds_read_b64_tr_b16 a[206:207], v231 offset:61440\n\t"
    "v_exp_f32 v156, v156\n\t"
    "v_mfma_f32_16x16x32_bf16 v[120:123], a[216:219], v[204:207], v[120:123]\n\t"
    "ds_read_b64_tr_b16 a[208:209], v232 offset:57344\n\t"
    "v_pk_mul_f32 v[32:33], v[32:33], v[252:253] op_sel_hi:[1,0]\n\t"
    "v_pk_mul_f32 v[34:35], v[34:35], v[252:253] op_sel_hi:[1,0]\n\t"
    "v_pk_mul_f32 v[36:37], v[36:37], v[252:253] op_sel_hi:[1,0]\n\t"
    "v_mfma_f32_16x16x32_bf16 v[92:95], a[220:223], v[200:203], v[92:95]\n\t"
    "ds_read_b64_tr_b16 a[210:211], v232 offset:61440\n\t"
    "v_exp_f32 v157, v157\n\t"
    "v_mfma_f32_16x16x32_bf16 v[124:127], a[220:223], v[204:207], v[124:127]\n\t"
    "ds_read_b64_tr_b16 a[212:213], v233 offset:57344\n\t"
    "v_cvt_pk_bf16_f32 v214, v156, v157\n\t"
    "v_pk_mul_f32 v[38:39], v[38:39], v[252:253] op_sel_hi:[1,0]\n\t"
    "v_cvt_pk_bf16_f32 v32, v32, v33\n\t"
    "v_cvt_pk_bf16_f32 v33, v34, v35\n\t"
    "v_mfma_f32_16x16x32_bf16 a[248:251], v[196:199], v[200:203], a[248:251]\n\t"
    "ds_read_b64_tr_b16 a[214:215], v233 offset:61440\n\t"
    "v_exp_f32 v158, v158\n\t"
    "v_mfma_f32_16x16x32_bf16 a[252:255], v[196:199], v[204:207], a[252:255]\n\t"
    "ds_read_b64_tr_b16 a[216:217], v234 offset:57344\n\t"
    "v_cvt_pk_bf16_f32 v34, v36, v37\n\t"
    "v_cvt_pk_bf16_f32 v35, v38, v39\n\t"
    "v_pk_mul_f32 v[40:41], v[40:41], v[252:253] op_sel_hi:[1,0]\n\t"
    "v_pk_mul_f32 v[42:43], v[42:43], v[252:253] op_sel_hi:[1,0]\n\t"
    "v_pk_mul_f32 v[44:45], v[44:45], v[252:253] op_sel_hi:[1,0]\n\t"
    "v_pk_mul_f32 v[46:47], v[46:47], v[252:253] op_sel_hi:[1,0]\n\t"
    "v_cvt_pk_bf16_f32 v40, v40, v41\n\t"
    "v_cvt_pk_bf16_f32 v41, v42, v43\n\t"
    "v_cvt_pk_bf16_f32 v42, v44, v45\n\t"
    "v_cvt_pk_bf16_f32 v43, v46, v47\n\t"
    "v_pk_mul_f32 v[48:49], v[48:49], v[252:253] op_sel_hi:[1,0]\n\t"
    "v_pk_mul_f32 v[50:51], v[50:51], v[252:253] op_sel_hi:[1,0]\n\t"
    "v_pk_mul_f32 v[52:53], v[52:53], v[252:253] op_sel_hi:[1,0]\n\t"
    "v_pk_mul_f32 v[54:55], v[54:55], v[252:253] op_sel_hi:[1,0]\n\t"
    "v_cvt_pk_bf16_f32 v48, v48, v49\n\t"
    "v_cvt_pk_bf16_f32 v49, v50, v51\n\t"
    "v_cvt_pk_bf16_f32 v50, v52, v53\n\t"
    "v_cvt_pk_bf16_f32 v51, v54, v55\n\t"
    "v_pk_mul_f32 v[56:57], v[56:57], v[252:253] op_sel_hi:[1,0]\n\t"
    "v_pk_mul_f32 v[58:59], v[58:59], v[252:253] op_sel_hi:[1,0]\n\t"
    "v_pk_mul_f32 v[60:61], v[60:61], v[252:253] op_sel_hi:[1,0]\n\t"
    "v_pk_mul_f32 v[62:63], v[62:63], v[252:253] op_sel_hi:[1,0]\n\t"
    "v_cvt_pk_bf16_f32 v56, v56, v57\n\t"
    "v_cvt_pk_bf16_f32 v57, v58, v59\n\t"
    "v_cvt_pk_bf16_f32 v58, v60, v61\n\t"
    "v_cvt_pk_bf16_f32 v59, v62, v63\n\t"
    "v_permlane16_swap_b32 v32, v34\n\t"
    "v_permlane16_swap_b32 v33, v35\n\t"
    "v_permlane16_swap_b32 v40, v42\n\t"
    "v_permlane16_swap_b32 v41, v43\n\t"
    "v_permlane16_swap_b32 v48, v50\n\t"
    "v_permlane16_swap_b32 v49, v51\n\t"
    "v_permlane16_swap_b32 v56, v58\n\t"
    "v_permlane16_swap_b32 v57, v59\n\t"
    "s_waitcnt lgkmcnt(8)\n\t"
    "buffer_store_dwordx4 v[4:7], v238, s[76:79], s90 offen offset:0 nt\n\t"
    "buffer_store_dwordx4 v[12:15], v238, s[76:79], s90 offen offset:1024 nt\n\t"
    "s_waitcnt lgkmcnt(6)\n\t"
    "buffer_store_dwordx4 v[20:23], v238, s[76:79], s90 offen offset:2048 nt\n\t"
    "buffer_store_dwordx4 v[28:31], v238, s[76:79], s90 offen offset:3072 nt\n\t"
    "ds_write_b128 v246, v[32:35] offset:0\n\t"
    "ds_write_b128 v247, v[40:43] offset:0\n\t"
    "ds_write_b128 v248, v[48:51] offset:0\n\t"
    "ds_write_b128 v249, v[56:59] offset:0\n\t"
    "ds_read_b128 v[36:39], v236 offset:0\n\t"
    "ds_read_b128 v[44:47], v237 offset:1024\n\t"
    "ds_read_b128 v[52:55], v236 offset:2048\n\t"
    "ds_read_b128 v[60:63], v237 offset:3072\n\t"
    "v_and_b32 v240, 15, v255\n\t"
    "v_lshrrev_b32 v241, 4, v255\n\t"
    "v_and_b32 v242, 7, v240\n\t"
    "v_xor_b32 v242, v242, v241\n\t"
    "v_lshlrev_b32 v243, 9, v240\n\t"
    "v_lshl_add_u32 v242, v242, 4, v243\n\t"
    "s_and_b32 s83, %[w], 1\n\t"
    "s_lshl_b32 s83, s83, 14\n\t"
    "s_lshr_b32 s94, %[w], 1\n\t"
    "s_lshl_b32 s94, s94, 16\n\t"
    "s_add_u32 s83, s83, s94\n\t"
    "s_add_u32 s83, s83, %[lds]\n\t"
    "v_add_u32 v242, s83, v242\n\t"
    "v_xor_b32 v243, 64, v242\n\t"
    "ds_write_b128 v242, a[0:3] offset:0\n\t"
    "ds_write_b128 v243, a[4:7] offset:0\n\t"
    "ds_write_b128 v242, a[8:11] offset:128\n\t"
    "ds_write_b128 v243, a[12:15] offset:128\n\t"
    "ds_write_b128 v242, a[16:19] offset:256\n\t"
    "ds_write_b128 v243, a[20:23] offset:256\n\t"
    "ds_write_b128 v242, a[24:27] offset:384\n\t"
    "ds_write_b128 v243, a[28:31] offset:384\n\t"
    "ds_write_b128 v242, a[32:35] offset:8192\n\t"
    "ds_write_b128 v243, a[36:39] offset:8192\n\t"
    "ds_write_b128 v242, a[40:43] offset:8320\n\t"
    "ds_write_b128 v243, a[44:47] offset:8320\n\t"
    "ds_write_b128 v242, a[48:51] offset:8448\n\t"
    "ds_write_b128 v243, a[52:55] offset:8448\n\t"
    "ds_write_b128 v242, a[56:59] offset:8576\n\t"
    "ds_write_b128 v243, a[60:63] offset:8576\n\t"
    "v_cmp_ngt_f32_e64 s[94:95], s87, v216\n\t"
    "s_nop 3\n\t"
    "s_or_b64 s[88:89], s[88:89], s[94:95]\n\t"
    "v_cmp_ngt_f32_e64 s[94:95], s87, v220\n\t"
    "s_nop 3\n\t"
    "s_or_b64 s[88:89], s[88:89], s[94:95]\n\t"
    "v_lshlrev_b32 v240, 2, v240\n\t"
    "s_lshl_b32 s83, %[w], 7\n\t"
    "s_add_u32 s83, s83, %[lds]\n\t"
    "s_add_u32 s83, s83, 147456\n\t"
    "v_add_u32 v240, s83, v240\n\t"
    "ds_write_b32 v240, v192 offset:0\n\t"
    "ds_write_b32 v240, v216 offset:512\n\t"
    "ds_write_b32 v240, v193 offset:64\n\t"
    "ds_write_b32 v240, v220 offset:576\n\t"
    "s_waitcnt lgkmcnt(15)\n\t"
    "buffer_store_dwordx4 v[36:39], v238, s[76:79], s91 offen offset:0 nt\n\t"
    "buffer_store_dwordx4 v[44:47], v238, s[76:79], s91 offen offset:1024 nt\n\t"
    "s_waitcnt lgkmcnt(15)\n\t"
    "buffer_store_dwordx4 v[52:55], v238, s[76:79], s91 offen offset:2048 nt\n\t"
    "buffer_store_dwordx4 v[60:63], v238, s[76:79], s91 offen offset:3072 nt\n\t"
    "s_waitcnt lgkmcnt(0)\n\t"
    "s_barrier\n\t"
    "v_mfma_f32_16x16x32_bf16 v[64:67], a[192:195], v[208:211], v[64:67]\n\t"
    "ds_read_b64_tr_b16 a[218:219], v234 offset:61440\n\t"
    "v_exp_f32 v159, v159\n\t"
    "v_mfma_f32_16x16x32_bf16 v[68:71], a[196:199], v[208:211], v[68:71]\n\t"
    "ds_read_b64_tr_b16 a[220:221], v235 offset:57344\n\t"
    "v_cvt_pk_bf16_f32 v215, v158, v159\n\t"
    "v_lshrrev_b32 v240, 4, v255\n\t"
    "s_lshl_b32 s83, %[w], 3\n\t"
    "v_add_u32 v240, s83, v240\n\t"
    "v_mfma_f32_16x16x32_bf16 v[72:75], a[200:203], v[208:211], v[72:75]\n\t"
    "ds_read_b64_tr_b16 a[222:223], v235 offset:61440\n\t"
    "v_and_b32 v241, 15, v255\n\t"
    "v_lshlrev_b32 v241, 1, v241\n\t"
    "v_and_b32 v242, 7, v240\n\t"
    "v_mfma_f32_16x16x32_bf16 v[76:79], a[204:207], v[208:211], v[76:79]\n\t"
    "v_xor_b32 v242, v242, v241\n\t"
    "v_lshlrev_b32 v243, 9, v240\n\t"
    "v_lshl_add_u32 v243, v242, 4, v243\n\t"
    "v_mfma_f32_16x16x32_bf16 v[80:83], a[208:211], v[208:211], v[80:83]\n\t"
    "v_add_u32 v243, %[lds], v243\n\t"
    "v_xor_b32 v242, 16, v243\n\t"
    "ds_read_b128 v[0:3], v243 offset:0\n\t"
    "v_mfma_f32_16x16x32_bf16 v[84:87], a[212:215], v[208:211], v[84:87]\n\t"
    "ds_read_b128 v[4:7], v242 offset:0\n\t"
    "ds_read_b128 v[8:11], v243 offset:16384\n\t"
    "ds_read_b128 v[12:15], v242 offset:16384\n\t"
    "s_waitcnt lgkmcnt(5)\n\t"
    "v_mfma_f32_16x16x32_bf16 v[88:91], a[216:219], v[208:211], v[88:91]\n\t"
    "s_mov_b32 s83, 65536\n\t"
    "v_add_u32 v243, s83, v243\n\t"
    "v_add_u32 v242, s83, v242\n\t"
    "s_waitcnt lgkmcnt(4)\n\t"
    "v_mfma_f32_16x16x32_bf16 v[92:95], a[220:223], v[208:211], v[92:95]\n\t"
    "ds_read_b128 v[16:19], v243 offset:0\n\t"
    "ds_read_b128 v[20:23], v242 offset:0\n\t"
    "ds_read_b128 v[24:27], v243 offset:16384\n\t"
    "v_mfma_f32_16x16x32_bf16 v[96:99], a[192:195], v[212:215], v[96:99]\n\t"
    "ds_read_b128 v[28:31], v242 offset:16384\n\t"
    "v_lshlrev_b32 v243, 2, v240\n\t"
    "s_add_u32 s83, %[lds], 147456\n\t"
    "v_mfma_f32_16x16x32_bf16 v[100:103], a[196:199], v[212:215], v[100:103]\n\t"
    "v_add_u32 v243, s83, v243\n\t"
    "ds_read_b32 v176, v243 offset:0\n\t"
    "ds_read_b32 v180, v243 offset:512\n\t"
    "v_mfma_f32_16x16x32_bf16 v[104:107], a[200:203], v[212:215], v[104:107]\n\t"
    "ds_read_b32 v177, v243 offset:128\n\t"
    "ds_read_b32 v181, v243 offset:640\n\t"
    "ds_read_b32 v178, v243 offset:256\n\t"
    "v_mfma_f32_16x16x32_bf16 v[108:111], a[204:207], v[212:215], v[108:111]\n\t"
    "ds_read_b32 v182, v243 offset:768\n\t"
    "ds_read_b32 v179, v243 offset:384\n\t"
    "ds_read_b32 v183, v243 offset:896\n\t"
    "v_mfma_f32_16x16x32_bf16 v[112:115], a[208:211], v[212:215], v[112:115]\n\t"
    "v_add_u32 v240, 4, v240\n\t"
    "v_and_b32 v242, 7, v240\n\t"
    "v_xor_b32 v242, v242, v241\n\t"
    "v_mfma_f32_16x16x32_bf16 v[116:119], a[212:215], v[212:215], v[116:119]\n\t"
    "v_lshlrev_b32 v243, 9, v240\n\t"
    "v_lshl_add_u32 v243, v242, 4, v243\n\t"
    "v_add_u32 v243, %[lds], v243\n\t"
    "v_mfma_f32_16x16x32_bf16 v[120:123], a[216:219], v[212:215], v[120:123]\n\t"
    "v_xor_b32 v242, 16, v243\n\t"
    "ds_read_b128 v[32:35], v243 offset:0\n\t"
    "ds_read_b128 v[36:39], v242 offset:0\n\t"
    "v_mfma_f32_16x16x32_bf16 v[124:127], a[220:223], v[212:215], v[124:127]\n\t"
    "ds_read_b128 v[40:43], v243 offset:16384\n\t"
    "ds_read_b128 v[44:47], v242 offset:16384\n\t"
    "s_mov_b32 s83, 65536\n\t"
    "v_mfma_f32_16x16x32_bf16 a[248:251], v[196:199], v[208:211], a[248:251]\n\t"
    "v_add_u32 v243, s83, v243\n\t"
    "v_add_u32 v242, s83, v242\n\t"
    "ds_read_b128 v[48:51], v243 offset:0\n\t"
    "v_mfma_f32_16x16x32_bf16 a[252:255], v[196:199], v[212:215], a[252:255]\n\t"
    "ds_read_b128 v[52:55], v242 offset:0\n\t"
    "ds_read_b128 v[56:59], v243 offset:16384\n\t"
    "ds_read_b128 v[60:63], v242 offset:16384\n\t"
    "v_lshlrev_b32 v243, 2, v240\n\t"
    "s_add_u32 s83, %[lds], 147456\n\t"
    "v_add_u32 v243, s83, v243\n\t"
    "ds_read_b32 v216, v243 offset:0\n\t"
    "ds_read_b32 v220, v243 offset:512\n\t"
    "ds_read_b32 v217, v243 offset:128\n\t"
    "ds_read_b32 v221, v243 offset:640\n\t"
    "ds_read_b32 v218, v243 offset:256\n\t"
    "ds_read_b32 v222, v243 offset:768\n\t"
    "ds_read_b32 v219, v243 offset:384\n\t"
    "ds_read_b32 v223, v243 offset:896\n\t"
    "s_waitcnt lgkmcnt(0)\n\t"
    "v_min_f32 v244, v176, v177\n\t"
    "v_min_f32 v245, v178, v179\n\t"
    "v_min_f32 v244, v244, v245\n\t"
    "v_sub_f32 v176, v244, v176\n\t"
    "v_sub_f32 v177, v244, v177\n\t"
    "v_sub_f32 v178, v244, v178\n\t"
    "v_sub_f32 v179, v244, v179\n\t"
    "v_exp_f32 v176, v176\n\t"
    "v_exp_f32 v177, v177\n\t"
    "v_exp_f32 v178, v178\n\t"
    "v_exp_f32 v179, v179\n\t"
    "s_nop 0\n\t"
    "v_mul_f32 v245, v176, v180\n\t"
    "v_fma_f32 v245, v177, v181, v245\n\t"
    "v_fma_f32 v245, v178, v182, v245\n\t"
    "v_fma_f32 v245, v179, v183, v245\n\t"
    "v_rcp_f32 v245, v245\n\t"
    "v_mul_f32 v0, v176, v0\n\t"
    "v_mul_f32 v1, v176, v1\n\t"
    "v_mul_f32 v2, v176, v2\n\t"
    "v_mul_f32 v3, v176, v3\n\t"
    "v_mul_f32 v4, v176, v4\n\t"
    "v_mul_f32 v5, v176, v5\n\t"
    "v_mul_f32 v6, v176, v6\n\t"
    "v_mul_f32 v7, v176, v7\n\t"
    "v_fma_f32 v0, v177, v8, v0\n\t"
    "v_fma_f32 v1, v177, v9, v1\n\t"
    "v_fma_f32 v2, v177, v10, v2\n\t"
    "v_fma_f32 v3, v177, v11, v3\n\t"
    "v_fma_f32 v4, v177, v12, v4\n\t"
    "v_fma_f32 v5, v177, v13, v5\n\t"
    "v_fma_f32 v6, v177, v14, v6\n\t"
    "v_fma_f32 v7, v177, v15, v7\n\t"
    "v_fma_f32 v0, v178, v16, v0\n\t"
    "v_fma_f32 v1, v178, v17, v1\n\t"
    "v_fma_f32 v2, v178, v18, v2\n\t"
    "v_fma_f32 v3, v178, v19, v3\n\t"
    "v_fma_f32 v4, v178, v20, v4\n\t"
    "v_fma_f32 v5, v178, v21, v5\n\t"
    "v_fma_f32 v6, v178, v22, v6\n\t"
    "v_fma_f32 v7, v178, v23, v7\n\t"
    "v_fma_f32 v0, v179, v24, v0\n\t"
    "v_fma_f32 v1, v179, v25, v1\n\t"
    "v_fma_f32 v2, v179, v26, v2\n\t"
    "v_fma_f32 v3, v179, v27, v3\n\t"
    "v_fma_f32 v4, v179, v28, v4\n\t"
    "v_fma_f32 v5, v179, v29, v5\n\t"
    "v_fma_f32 v6, v179, v30, v6\n\t"
    "v_fma_f32 v7, v179, v31, v7\n\t"
    "v_mul_f32 v0, v245, v0\n\t"
    "v_mul_f32 v1, v245, v1\n\t"
    "v_mul_f32 v2, v245, v2\n\t"
    "v_mul_f32 v3, v245, v3\n\t"
    "v_mul_f32 v4, v245, v4\n\t"
    "v_mul_f32 v5, v245, v5\n\t"
    "v_mul_f32 v6, v245, v6\n\t"
    "v_mul_f32 v7, v245, v7\n\t"
    "v_cvt_pk_bf16_f32 v8, v0, v1\n\t"
    "v_cvt_pk_bf16_f32 v9, v2, v3\n\t"
    "v_cvt_pk_bf16_f32 v10, v4, v5\n\t"
    "v_cvt_pk_bf16_f32 v11, v6, v7\n\t"
    "v_min_f32 v244, v216, v217\n\t"
    "v_min_f32 v245, v218, v219\n\t"
    "v_min_f32 v244, v244, v245\n\t"
    "v_sub_f32 v216, v244, v216\n\t"
    "v_sub_f32 v217, v244, v217\n\t"
    "v_sub_f32 v218, v244, v218\n\t"
    "v_sub_f32 v219, v244, v219\n\t"
    "v_exp_f32 v216, v216\n\t"
    "v_exp_f32 v217, v217\n\t"
    "v_exp_f32 v218, v218\n\t"
    "v_exp_f32 v219, v219\n\t"
    "s_nop 0\n\t"
    "v_mul_f32 v245, v216, v220\n\t"
    "v_fma_f32 v245, v217, v221, v245\n\t"
    "v_fma_f32 v245, v218, v222, v245\n\t"
    "v_fma_f32 v245, v219, v223, v245\n\t"
    "v_rcp_f32 v245, v245\n\t"
    "v_mul_f32 v32, v216, v32\n\t"
    "v_mul_f32 v33, v216, v33\n\t"
    "v_mul_f32 v34, v216, v34\n\t"
    "v_mul_f32 v35, v216, v35\n\t"
    "v_mul_f32 v36, v216, v36\n\t"
    "v_mul_f32 v37, v216, v37\n\t"
    "v_mul_f32 v38, v216, v38\n\t"
    "v_mul_f32 v39, v216, v39\n\t"
    "v_fma_f32 v32, v217, v40, v32\n\t"
    "v_fma_f32 v33, v217, v41, v33\n\t"
    "v_fma_f32 v34, v217, v42, v34\n\t"
    "v_fma_f32 v35, v217, v43, v35\n\t"
    "v_fma_f32 v36, v217, v44, v36\n\t"
    "v_fma_f32 v37, v217, v45, v37\n\t"
    "v_fma_f32 v38, v217, v46, v38\n\t"
    "v_fma_f32 v39, v217, v47, v39\n\t"
    "v_fma_f32 v32, v218, v48, v32\n\t"
    "v_fma_f32 v33, v218, v49, v33\n\t"
    "v_fma_f32 v34, v218, v50, v34\n\t"
    "v_fma_f32 v35, v218, v51, v35\n\t"
    "v_fma_f32 v36, v218, v52, v36\n\t"
    "v_fma_f32 v37, v218, v53, v37\n\t"
    "v_fma_f32 v38, v218, v54, v38\n\t"
    "v_fma_f32 v39, v218, v55, v39\n\t"
    "v_fma_f32 v32, v219, v56, v32\n\t"
    "v_fma_f32 v33, v219, v57, v33\n\t"
    "v_fma_f32 v34, v219, v58, v34\n\t"
    "v_fma_f32 v35, v219, v59, v35\n\t"
    "v_fma_f32 v36, v219, v60, v36\n\t"
    "v_fma_f32 v37, v219, v61, v37\n\t"
    "v_fma_f32 v38, v219, v62, v38\n\t"
    "v_fma_f32 v39, v219, v63, v39\n\t"
    "v_mul_f32 v32, v245, v32\n\t"
    "v_mul_f32 v33, v245, v33\n\t"
    "v_mul_f32 v34, v245, v34\n\t"
    "v_mul_f32 v35, v245, v35\n\t"
    "v_mul_f32 v36, v245, v36\n\t"
    "v_mul_f32 v37, v245, v37\n\t"
    "v_mul_f32 v38, v245, v38\n\t"
    "v_mul_f32 v39, v245, v39\n\t"
    "v_cvt_pk_bf16_f32 v40, v32, v33\n\t"
    "v_cvt_pk_bf16_f32 v41, v34, v35\n\t"
    "v_cvt_pk_bf16_f32 v42, v36, v37\n\t"
    "v_cvt_pk_bf16_f32 v43, v38, v39\n\t"
    "s_lshl_b32 s83, %[w], 11\n\t"
    "s_add_u32 s83, s83, 65536\n\t"
    "buffer_store_dwordx4 v[8:11], v238, s[76:79], s83 offen offset:0 nt\n\t"
    "buffer_store_dwordx4 v[40:43], v238, s[76:79], s83 offen offset:1024 nt\n\t"
    "s_nop 7\n\t"
    "v_accvgpr_read_b32 v244, a248\n\t"
    "v_accvgpr_read_b32 v245, a252\n\t"
    "v_rcp_f32 v250, v244\n\t"
    "v_rcp_f32 v252, v245\n\t"
    "v_cmp_ngt_f32_e64 s[94:95], s87, v244\n\t"
    "s_nop 3\n\t"
    "s_or_b64 s[88:89], s[88:89], s[94:95]\n\t"
    "v_cmp_ngt_f32_e64 s[94:95], s87, v245\n\t"
    "s_nop 3\n\t"
    "s_or_b64 s[88:89], s[88:89], s[94:95]\n\t"
    "s_add_u32 s92, s82, 8192\n\t"
    "s_add_u32 s93, s82, 12288\n\t"
    "v_pk_mul_f32 v[64:65], v[64:65], v[250:251] op_sel_hi:[1,0]\n\t"
    "v_pk_mul_f32 v[66:67], v[66:67], v[250:251] op_sel_hi:[1,0]\n\t"
    "v_pk_mul_f32 v[68:69], v[68:69], v[250:251] op_sel_hi:[1,0]\n\t"
    "v_pk_mul_f32 v[70:71], v[70:71], v[250:251] op_sel_hi:[1,0]\n\t"
    "v_cvt_pk_bf16_f32 v64, v64, v65\n\t"
    "v_cvt_pk_bf16_f32 v65, v66, v67\n\t"
    "v_cvt_pk_bf16_f32 v66, v68, v69\n\t"
    "v_cvt_pk_bf16_f32 v67, v70, v71\n\t"
    "v_pk_mul_f32 v[72:73], v[72:73], v[250:251] op_sel_hi:[1,0]\n\t"
    "v_pk_mul_f32 v[74:75], v[74:75], v[250:251] op_sel_hi:[1,0]\n\t"
    "v_pk_mul_f32 v[76:77], v[76:77], v[250:251] op_sel_hi:[1,0]\n\t"
    "v_pk_mul_f32 v[78:79], v[78:79], v[250:251] op_sel_hi:[1,0]\n\t"
    "v_cvt_pk_bf16_f32 v72, v72, v73\n\t"
    "v_cvt_pk_bf16_f32 v73, v74, v75\n\t"
    "v_cvt_pk_bf16_f32 v74, v76, v77\n\t"
    "v_cvt_pk_bf16_f32 v75, v78, v79\n\t"
    "v_pk_mul_f32 v[80:81], v[80:81], v[250:251] op_sel_hi:[1,0]\n\t"
    "v_pk_mul_f32 v[82:83], v[82:83], v[250:251] op_sel_hi:[1,0]\n\t"
    "v_pk_mul_f32 v[84:85], v[84:85], v[250:251] op_sel_hi:[1,0]\n\t"
    "v_pk_mul_f32 v[86:87], v[86:87], v[250:251] op_sel_hi:[1,0]\n\t"
    "v_cvt_pk_bf16_f32 v80, v80, v81\n\t"
    "v_cvt_pk_bf16_f32 v81, v82, v83\n\t"
    "v_cvt_pk_bf16_f32 v82, v84, v85\n\t"
    "v_cvt_pk_bf16_f32 v83, v86, v87\n\t"
    "v_pk_mul_f32 v[88:89], v[88:89], v[250:251] op_sel_hi:[1,0]\n\t"
    "v_pk_mul_f32 v[90:91], v[90:91], v[250:251] op_sel_hi:[1,0]\n\t"
    "v_pk_mul_f32 v[92:93], v[92:93], v[250:251] op_sel_hi:[1,0]\n\t"
    "v_pk_mul_f32 v[94:95], v[94:95], v[250:251] op_sel_hi:[1,0]\n\t"
    "v_cvt_pk_bf16_f32 v88, v88, v89\n\t"
    "v_cvt_pk_bf16_f32 v89, v90, v91\n\t"
    "v_cvt_pk_bf16_f32 v90, v92, v93\n\t"
    "v_cvt_pk_bf16_f32 v91, v94, v95\n\t"
    "v_permlane16_swap_b32 v64, v66\n\t"
    "v_permlane16_swap_b32 v65, v67\n\t"
    "v_permlane16_swap_b32 v72, v74\n\t"
    "v_permlane16_swap_b32 v73, v75\n\t"
    "v_permlane16_swap_b32 v80, v82\n\t"
    "v_permlane16_swap_b32 v81, v83\n\t"
    "v_permlane16_swap_b32 v88, v90\n\t"
    "v_permlane16_swap_b32 v89, v91\n\t"
    "ds_write_b128 v246, v[64:67] offset:0\n\t"
    "ds_write_b128 v247, v[72:75] offset:0\n\t"
    "ds_write_b128 v248, v[80:83] offset:0\n\t"
    "ds_write_b128 v249, v[88:91] offset:0\n\t"
    "ds_read_b128 v[68:71], v236 offset:0\n\t"
    "ds_read_b128 v[76:79], v237 offset:1024\n\t"
    "ds_read_b128 v[84:87], v236 offset:2048\n\t"
    "ds_read_b128 v[92:95], v237 offset:3072\n\t"
    "v_pk_mul_f32 v[96:97], v[96:97], v[252:253] op_sel_hi:[1,0]\n\t"
    "v_pk_mul_f32 v[98:99], v[98:99], v[252:253] op_sel_hi:[1,0]\n\t"
    "v_pk_mul_f32 v[100:101], v[100:101], v[252:253] op_sel_hi:[1,0]\n\t"
    "v_pk_mul_f32 v[102:103], v[102:103], v[252:253] op_sel_hi:[1,0]\n\t"
    "v_cvt_pk_bf16_f32 v96, v96, v97\n\t"
    "v_cvt_pk_bf16_f32 v97, v98, v99\n\t"
    "v_cvt_pk_bf16_f32 v98, v100, v101\n\t"
    "v_cvt_pk_bf16_f32 v99, v102, v103\n\t"
    "v_pk_mul_f32 v[104:105], v[104:105], v[252:253] op_sel_hi:[1,0]\n\t"
    "v_pk_mul_f32 v[106:107], v[106:107], v[252:253] op_sel_hi:[1,0]\n\t"
    "v_pk_mul_f32 v[108:109], v[108:109], v[252:253] op_sel_hi:[1,0]\n\t"
    "v_pk_mul_f32 v[110:111], v[110:111], v[252:253] op_sel_hi:[1,0]\n\t"
    "v_cvt_pk_bf16_f32 v104, v104, v105\n\t"
    "v_cvt_pk_bf16_f32 v105, v106, v107\n\t"
    "v_cvt_pk_bf16_f32 v106, v108, v109\n\t"
    "v_cvt_pk_bf16_f32 v107, v110, v111\n\t"
    "v_pk_mul_f32 v[112:113], v[112:113], v[252:253] op_sel_hi:[1,0]\n\t"
    "v_pk_mul_f32 v[114:115], v[114:115], v[252:253] op_sel_hi:[1,0]\n\t"
    "v_pk_mul_f32 v[116:117], v[116:117], v[252:253] op_sel_hi:[1,0]\n\t"
    "v_pk_mul_f32 v[118:119], v[118:119], v[252:253] op_sel_hi:[1,0]\n\t"
    "v_cvt_pk_bf16_f32 v112, v112, v113\n\t"
    "v_cvt_pk_bf16_f32 v113, v114, v115\n\t"
    "v_cvt_pk_bf16_f32 v114, v116, v117\n\t"
    "v_cvt_pk_bf16_f32 v115, v118, v119\n\t"
    "v_pk_mul_f32 v[120:121], v[120:121], v[252:253] op_sel_hi:[1,0]\n\t"
    "v_pk_mul_f32 v[122:123], v[122:123], v[252:253] op_sel_hi:[1,0]\n\t"
    "v_pk_mul_f32 v[124:125], v[124:125], v[252:253] op_sel_hi:[1,0]\n\t"
    "v_pk_mul_f32 v[126:127], v[126:127], v[252:253] op_sel_hi:[1,0]\n\t"
    "v_cvt_pk_bf16_f32 v120, v120, v121\n\t"
    "v_cvt_pk_bf16_f32 v121, v122, v123\n\t"
    "v_cvt_pk_bf16_f32 v122, v124, v125\n\t"
    "v_cvt_pk_bf16_f32 v123, v126, v127\n\t"
    "v_permlane16_swap_b32 v96, v98\n\t"
    "v_permlane16_swap_b32 v97, v99\n\t"
    "v_permlane16_swap_b32 v104, v106\n\t"
    "v_permlane16_swap_b32 v105, v107\n\t"
    "v_permlane16_swap_b32 v112, v114\n\t"
    "v_permlane16_swap_b32 v113, v115\n\t"
    "v_permlane16_swap_b32 v120, v122\n\t"
    "v_permlane16_swap_b32 v121, v123\n\t"
    "s_waitcnt lgkmcnt(2)\n\t"
    "buffer_store_dwordx4 v[68:71], v238, s[76:79], s92 offen offset:0 nt\n\t"
    "buffer_store_dwordx4 v[76:79], v238, s[76:79], s92 offen offset:1024 nt\n\t"
    "s_waitcnt lgkmcnt(0)\n\t"
    "buffer_store_dwordx4 v[84:87], v238, s[76:79], s92 offen offset:2048 nt\n\t"
    "buffer_store_dwordx4 v[92:95], v238, s[76:79], s92 offen offset:3072 nt\n\t"
    "ds_write_b128 v246, v[96:99] offset:0\n\t"
    "ds_write_b128 v247, v[104:107] offset:0\n\t"
    "ds_write_b128 v248, v[112:115] offset:0\n\t"
    "ds_write_b128 v249, v[120:123] offset:0\n\t"
    "ds_read_b128 v[100:103], v236 offset:0\n\t"
    "ds_read_b128 v[108:111], v237 offset:1024\n\t"
    "ds_read_b128 v[116:119], v236 offset:2048\n\t"
    "ds_read_b128 v[124:127], v237 offset:3072\n\t"
    "s_waitcnt lgkmcnt(2)\n\t"
    "buffer_store_dwordx4 v[100:103], v238, s[76:79], s93 offen offset:0 nt\n\t"
    "buffer_store_dwordx4 v[108:111], v238, s[76:79], s93 offen offset:1024 nt\n\t"
    "s_waitcnt lgkmcnt(0)\n\t"
    "buffer_store_dwordx4 v[116:119], v238, s[76:79], s93 offen offset:2048 nt\n\t"
    "buffer_store_dwordx4 v[124:127], v238, s[76:79], s93 offen offset:3072 nt\n\t"
    "s_cmp_lg_u64 s[88:89], 0\n\t"
    "s_cselect_b32 s94, 1, 0\n\t"
    "s_lshl_b32 s83, %[w], 2\n\t"
    "s_add_u32 s83, s83, %[lds]\n\t"
    "s_add_u32 s83, s83, 148480\n\t"
    "v_mov_b32 v240, s83\n\t"
    "v_mov_b32 v241, s94\n\t"
    "ds_write_b32 v240, v241 offset:0\n\t"
    "s_add_u32 s83, %[lds], 148480\n\t"
    "v_mov_b32 v240, s83\n\t"
    "s_waitcnt lgkmcnt(0)\n\t"
    "s_barrier\n\t"
    "ds_read_b128 v[244:247], v240 offset:0\n\t"
    "s_waitcnt lgkmcnt(0)\n\t"
    "v_or_b32 v244, v244, v245\n\t"
    "v_or_b32 v246, v246, v247\n\t"
    "v_or_b32 v244, v244, v246\n\t"
    "s_nop 0\n\t"
    "v_readfirstlane_b32 s94, v244\n\t"
    "s_nop 3\n\t"
    "s_mov_b32 %[fail], s94\n\t"
    "LEND:\n\t"
    : [fail] "=&s"(fail)
    : [qlo] "s"((unsigned)qa), [qhi] "s"((unsigned)(qa >> 32)), [klo] "s"((unsigned)ka), [khi] "s"((unsigned)(ka >> 32)),
      [vlo] "s"((unsigned)va), [vhi] "s"((unsigned)(va >> 32)), [olo] "s"((unsigned)oa), [ohi] "s"((unsigned)(oa >> 32)),
      [w] "s"(w), [lds] "s"(ldsb), [bid] "s"(bh)
    : "v0", "v1", "v2", "v3", "v4", "v5", "v6", "v7", "v8", "v9", "v10", "v11", "v12", "v13", "v14", "v15", "v16", "v17", "v18", "v19", "v20", "v21", "v22", "v23", "v24", "v25", "v26", "v27", "v28", "v29", "v30", "v31", "v32", "v33", "v34", "v35", "v36", "v37", "v38", "v39", "v40", "v41", "v42", "v43", "v44", "v45", "v46", "v47", "v48", "v49", "v50", "v51", "v52", "v53", "v54", "v55", "v56", "v57", "v58", "v59", "v60", "v61", "v62", "v63", "v64", "v65", "v66", "v67", "v68", "v69", "v70", "v71", "v72", "v73", "v74", "v75", "v76", "v77", "v78", "v79", "v80", "v81", "v82", "v83", "v84", "v85", "v86", "v87", "v88", "v89", "v90", "v91", "v92", "v93", "v94", "v95", "v96", "v97", "v98", "v99", "v100", "v101", "v102", "v103", "v104", "v105", "v106", "v107", "v108", "v109", "v110", "v111", "v112", "v113", "v114", "v115", "v116", "v117", "v118", "v119", "v120", "v121", "v122", "v123", "v124", "v125", "v126", "v127", "v128", "v129", "v130", "v131", "v132", "v133", "v134", "v135", "v136", "v137", "v138", "v139", "v140", "v141", "v142", "v143", "v144", "v145", "v146", "v147", "v148", "v149", "v150", "v151", "v152", "v153", "v154", "v155", "v156", "v157", "v158", "v159", "v160", "v161", "v162", "v163", "v164", "v165", "v166", "v167", "v168", "v169", "v170", "v171", "v172", "v173", "v174", "v175", "v176", "v177", "v178", "v179", "v180", "v181", "v182", "v183", "v184", "v185", "v186", "v187", "v188", "v189", "v190", "v191", "v192", "v193", "v194", "v195", "v196", "v197", "v198", "v199", "v200", "v201", "v202", "v203", "v204", "v205", "v206", "v207", "v208", "v209", "v210", "v211", "v212", "v213", "v214", "v215", "v216", "v217", "v218", "v219", "v220", "v221", "v222", "v223", "v224", "v225", "v226", "v227", "v228", "v229", "v230", "v231", "v232", "v233", "v234", "v235", "v236", "v237", "v238", "v239", "v240", "v241", "v242", "v243", "v244", "v245", "v246", "v247", "v248", "v249", "v250", "v251", "v252", "v253", "v254", "v255", "a0", "a1", "a2", "a3", "a4", "a5", "a6", "a7", "a8", "a9", "a10", "a11", "a12", "a13", "a14", "a15", "a16", "a17", "a18", "a19", "a20", "a21", "a22", "a23", "a24", "a25", "a26", "a27", "a28", "a29", "a30", "a31", "a32", "a33", "a34", "a35", "a36", "a37", "a38", "a39", "a40", "a41", "a42", "a43", "a44", "a45", "a46", "a47", "a48", "a49", "a50", "a51", "a52", "a53", "a54", "a55", "a56", "a57", "a58", "a59", "a60", "a61", "a62", "a63", "a64", "a65", "a66", "a67", "a68", "a69", "a70", "a71", "a72", "a73", "a74", "a75", "a76", "a77", "a78", "a79", "a80", "a81", "a82", "a83", "a84", "a85", "a86", "a87", "a88", "a89", "a90", "a91", "a92", "a93", "a94", "a95", "a96", "a97", "a98", "a99", "a100", "a101", "a102", "a103", "a104", "a105", "a106", "a107", "a108", "a109", "a110", "a111", "a112", "a113", "a114", "a115", "a116", "a117", "a118", "a119", "a120", "a121", "a122", "a123", "a124", "a125", "a126", "a127", "a128", "a129", "a130", "a131", "a132", "a133", "a134", "a135", "a136", "a137", "a138", "a139", "a140", "a141", "a142", "a143", "a144", "a145", "a146", "a147", "a148", "a149", "a150", "a151", "a152", "a153", "a154", "a155", "a156", "a157", "a158", "a159", "a160", "a161", "a162", "a163", "a164", "a165", "a166", "a167", "a168", "a169", "a170", "a171", "a172", "a173", "a174", "a175", "a176", "a177", "a178", "a179", "a180", "a181", "a182", "a183", "a184", "a185", "a186", "a187", "a188", "a189", "a190", "a191", "a192", "a193", "a194", "a195", "a196", "a197", "a198", "a199", "a200", "a201", "a202", "a203", "a204", "a205", "a206", "a207", "a208", "a209", "a210", "a211", "a212", "a213", "a214", "a215", "a216", "a217", "a218", "a219", "a220", "a221", "a222", "a223", "a224", "a225", "a226", "a227", "a228", "a229", "a230", "a231", "a232", "a233", "a234", "a235", "a236", "a237", "a238", "a239", "a240", "a241", "a242", "a243", "a244", "a245", "a246", "a247", "a248", "a249", "a250", "a251", "a252", "a253", "a254", "a255", "s63", "s64", "s65", "s66", "s67", "s68", "s69", "s70", "s71", "s72", "s73", "s74", "s75", "s76", "s77", "s78", "s79", "s80", "s81", "s82", "s83", "s84", "s85", "s86", "s87", "s88", "s89", "s90", "s91", "s92", "s93", "s94", "s95", "s96", "s97", "s98", "s99", "vcc", "scc", "memory");
  if (__builtin_expect(fail != 0, 0)) {
    asm volatile("s_waitcnt vmcnt(0) lgkmcnt(0)\n\ts_barrier" ::: "memory");
    int lane;
    asm volatile("v_mbcnt_lo_u32_b32 %0, -1, 0\n\tv_mbcnt_hi_u32_b32 %0, -1, %0" : "=v"(lane));
    char* my = lds + w * 8192;
    const unsigned mya = ldsb + w * 8192;
    const int nb = w == 0 ? 3 : 2;
#pragma unroll 1
    for (int b = 0; b < nb; ++b) slow_block((const char*)qa, (const char*)ka, (const char*)va, (char*)oa, b < 2 ? 64 * (int)w + 32 * b : 256, my, mya, lane);
  }
}
}

extern "C" void kernel_launch(void* const* d_in, const int* in_sizes, int n_in, void* d_out, int out_size, void* d_ws, size_t ws_size, hipStream_t stream) {
  (void)in_sizes; (void)n_in; (void)out_size; (void)d_ws; (void)ws_size;
  (void)hipFuncSetAttribute((const void*)gqa::attn_kernel, hipFuncAttributeMaxDynamicSharedMemorySize, 163840);
  hipLaunchKernelGGL(gqa::attn_kernel, dim3(gqa::kB * gqa::kHq), dim3(256), 163840, stream, (const char*)d_in[0], (const char*)d_in[1], (const char*)d_in[2], (char*)d_out
  );
}
```
